```python
import torch

def _softmax(x, axis=-1):
    return torch.softmax(x, dim=axis)
def _gelu(x):
    return torch.nn.functional.gelu(x, approximate="tanh")
def _silu(x):
    return torch.nn.functional.silu(x)
def _sigmoid(x):
    return torch.sigmoid(x)
def _layernorm(x, axis=-1, eps=1e-5):
    m = x.mean(dim=axis, keepdim=True)
    v = x.var(dim=axis, keepdim=True, unbiased=False)
    return (x - m) / torch.sqrt(v + eps)
def _scatter_add(tgt, idx, src, axis=0):
    return tgt.clone().index_add_(axis, idx, src)
def _segment_sum(src, seg, n):
    out = torch.zeros((n,) + src.shape[1:], dtype=src.dtype)
    return out.index_add_(0, seg, src)
def _pad(x, pw):
    flat = tuple(v for pair in reversed(pw) for v in pair)
    return torch.nn.functional.pad(x, flat)

kernel_name = 'interleave_layers__linear_kernel_hedgehog__mixer_s_mlp__prompt'

def setup_inputs():

    _w = (lambda a: (lambda t: t.to(torch.bfloat16) if t.is_floating_point() else t)(torch.as_tensor(a)))
    def _A_setup():
        B, S, D, H = 1, 2048, 448, 16
        Dh = D // H
        return {
            "x": _w((lambda *s: torch.randn(*s, dtype=torch.bfloat16) * 0.05)(B, S, D)),
            "wqkv": _w((lambda *s: torch.randn(*s, dtype=torch.bfloat16) * 0.4)(D, 3 * D)),
            "w_phi": _w((lambda *s: torch.randn(*s, dtype=torch.bfloat16) * 0.4)(H, Dh, Dh) * (Dh ** -0.5)),
            "wo": _w((lambda *s: torch.randn(*s, dtype=torch.bfloat16) * 0.05)(D, D)),
        }
    _A_in = _A_setup()

    def _B_setup():
        B, S, D, F = 1, 2048, 448, 1792
        return {
            "x":  _w((lambda *s: torch.randn(*s, dtype=torch.bfloat16) * 0.05)(B, S, D)),
            "w1": _w((lambda *s: torch.randn(*s, dtype=torch.bfloat16) * 3.2)(D, D) * (D ** -0.5)),
            "w2": _w((lambda *s: torch.randn(*s, dtype=torch.bfloat16) * 3.2)(D, D) * (D ** -0.5)),
            "w3": _w((lambda *s: torch.randn(*s, dtype=torch.bfloat16) * 3.2)(D, F) * (D ** -0.5)),
            "w4": _w((lambda *s: torch.randn(*s, dtype=torch.bfloat16) * 3.2)(F, D) * (F ** -0.5)),
        }
    _B_in = _B_setup()

    return {
        "A_x": _A_in["x"],
        "A_wqkv": _A_in["wqkv"],
        "A_w_phi": _A_in["w_phi"],
        "A_wo": _A_in["wo"],
        "B_x": _B_in["x"],
        "B_w1": _B_in["w1"],
        "B_w2": _B_in["w2"],
        "B_w3": _B_in["w3"],
        "B_w4": _B_in["w4"],
    }

def reference(A_x, A_wqkv, A_w_phi, A_wo, B_x, B_w1, B_w2, B_w3, B_w4):
    def _A_ref(x, wqkv, w_phi, wo):
        B, S, D = x.shape
        H = 16; Dh = D // H
        qkv = (lambda x, *d: x.permute(*d))((x @ wqkv).reshape(B, S, 3, H, Dh), 2, 0, 3, 1, 4)
        q, k, v = qkv[0], qkv[1], qkv[2]
        pq = torch.einsum("bhsd,hdf->bhsf", q, w_phi)
        pk = torch.einsum("bhsd,hdf->bhsf", k, w_phi)
        phi_q = torch.concatenate([_softmax(pq, axis=-1), _softmax(-pq, axis=-1)], axis=-1)
        phi_k = torch.concatenate([_softmax(pk, axis=-1), _softmax(-pk, axis=-1)], axis=-1)
        kv = torch.einsum("bhsf,bhsd->bhfd", phi_k, v)
        z  = torch.einsum("bhsf->bhf", phi_k)
        num = torch.einsum("bhqf,bhfd->bhqd", phi_q, kv)
        den = torch.einsum("bhqf,bhf->bhq", phi_q, z)[..., None] + 1e-6
        o = num / den
        return (lambda x, *d: x.permute(*d))(o, 0, 2, 1, 3).reshape(B, S, D) @ wo

    def _B_ref(x, w1, w2, w3, w4):
        B, S, D = x.shape
        h = _gelu(_layernorm(x, axis=-1) @ w1)
        c = D // 4
        g0 = _pad(h[:, :, 0*c:1*c], ((0, 0), (1, 0), (0, 0)))[:, :S, :]
        g1 = _pad(h[:, :, 1*c:2*c], ((0, 0), (0, 1), (0, 0)))[:, 1:, :]
        g2 = _pad(h[:, :, 2*c:3*c], ((0, 0), (2, 0), (0, 0)))[:, :S, :]
        g3 = _pad(h[:, :, 3*c:   ], ((0, 0), (0, 2), (0, 0)))[:, 2:, :]
        hs = torch.concatenate([g0, g1, g2, g3], axis=-1) @ w2
        x1 = x + hs
        h2 = _gelu(_layernorm(x1, axis=-1) @ w3) @ w4
        return x1 + h2

    x = A_x
    x = x + _A_ref(_layernorm(x, axis=-1), A_wqkv, A_w_phi, A_wo)
    x = x + _B_ref(_layernorm(x, axis=-1), B_w1, B_w2, B_w3, B_w4)
    x = x + _A_ref(_layernorm(x, axis=-1), A_wqkv, A_w_phi, A_wo)
    x = x + _B_ref(_layernorm(x, axis=-1), B_w1, B_w2, B_w3, B_w4)
    return x
```

```cpp
#include <hip/hip_runtime.h>
#include <hip/hip_bf16.h>
#include <stdint.h>

#define GAS __attribute__((address_space(1)))
#define LAS __attribute__((address_space(3)))
#define RLX_AGENT __ATOMIC_RELAXED, __HIP_MEMORY_SCOPE_AGENT
#define DI __device__ __forceinline__
typedef unsigned v4u __attribute__((ext_vector_type(4)));
typedef unsigned v2u __attribute__((ext_vector_type(2)));
typedef float f32x4 __attribute__((ext_vector_type(4)));
typedef float f32x2 __attribute__((ext_vector_type(2)));
typedef short bf16x8 __attribute__((ext_vector_type(8)));
typedef short bf16x4 __attribute__((ext_vector_type(4)));
typedef __bf16 bfv2 __attribute__((ext_vector_type(2)));
typedef GAS unsigned gu32;

namespace {

constexpr int S_ = 2048, D_ = 448, H_ = 16, DH = 28, F_ = 1792;
constexpr int NTHR = 512;
constexpr int TILE_D = (D_ / 8) * 256;
constexpr int TILE_F = (F_ / 8) * 256;
constexpr int ACT_D = 128 * TILE_D;
constexpr int ACT_F = 128 * TILE_F;

constexpr int CTRL_BYTES = 65536;
constexpr int C_TMO = 0;
constexpr int C_XN = 64 * 1, C_KV = 64 * 33, C_O = 64 * 49, C_XP = 64 * 65, C_H = 64 * 97, C_X1 = 64 * 129, C_G = 64 * 161;
constexpr int C_WORDS = 64 * 193;
constexpr int WQKV_T = CTRL_BYTES;
constexpr int WO_T = WQKV_T + 16 * 6 * 14 * 1024;
constexpr int W1_T = WO_T + 28 * 14 * 1024;
constexpr int W2_T = W1_T + 28 * 14 * 1024;
constexpr int W3_T = W2_T + 28 * 14 * 1024;
constexpr int W4_T = W3_T + 112 * 14 * 1024;
constexpr int WPHI_T = W4_T + 28 * 56 * 1024;
constexpr int W_END = WPHI_T + 16 * 2 * 1024;
constexpr int XIN = W_END;
constexpr int O_B = XIN + 2 * ACT_D;
constexpr int XP_B = O_B + 2 * ACT_D;
constexpr int H_B = XP_B + 2 * ACT_D;
constexpr int X1_B = H_B + 2 * ACT_D;
constexpr int G_B = X1_B + 2 * ACT_D;
constexpr int KVP_B = G_B + 2 * ACT_F;
constexpr int WS_END = KVP_B + 2 * 16 * 16 * 8192;

constexpr int L_A = 0;
constexpr int L_Y = 57344;
constexpr int L_RED = 0;
constexpr int L_WQ = 0;
constexpr int L_EX = 86016;
constexpr int L_KVS = 110592;
constexpr int L_FLAG = 131072;
constexpr int LDS_BYTES = 131072 + 256;

constexpr unsigned kSpinCap = 1u << 22;

struct Params {
  const unsigned short *x, *wqkv, *wphi, *wo, *w1, *w2, *w3, *w4;
  unsigned short *out;
  unsigned char *ws;
};

DI float bflo(unsigned u) { return __builtin_bit_cast(float, u << 16); }
DI float bfhi(unsigned u) { return __builtin_bit_cast(float, u & 0xffff0000u); }
DI unsigned pk2(float a, float b) {
  f32x2 v = {a, b};
  return __builtin_bit_cast(unsigned, __builtin_convertvector(v, bfv2));
}
DI float rbf(float a) { return (float)(__bf16)a; }
DI f32x4 mfma32(v4u a, v4u b, f32x4 c) {
  return __builtin_amdgcn_mfma_f32_16x16x32_bf16(__builtin_bit_cast(bf16x8, a), __builtin_bit_cast(bf16x8, b), c, 0, 0, 0);
}
DI f32x4 mfma16(v2u a, v2u b, f32x4 c) {
  return __builtin_amdgcn_mfma_f32_16x16x16bf16_1k(__builtin_bit_cast(bf16x4, a), __builtin_bit_cast(bf16x4, b), c, 0, 0, 0);
}
#define DRAIN() asm volatile("s_waitcnt vmcnt(0)" ::: "memory")
constexpr int SC1 = 16;
DI v4u ld16(__amdgpu_buffer_rsrc_t rs, int voff, int soff) { return __builtin_amdgcn_raw_buffer_load_b128(rs, voff, soff, SC1); }
DI v2u ld8(__amdgpu_buffer_rsrc_t rs, int voff, int soff) { return __builtin_amdgcn_raw_buffer_load_b64(rs, voff, soff, SC1); }
DI v4u ldw(__amdgpu_buffer_rsrc_t rs, int voff, int soff) { return __builtin_amdgcn_raw_buffer_load_b128(rs, voff, soff, SC1); }
DI void st16(__amdgpu_buffer_rsrc_t rs, int voff, int soff, v4u v) { __builtin_amdgcn_raw_buffer_store_b128(v, rs, voff, soff, SC1); }
DI void st8(__amdgpu_buffer_rsrc_t rs, int voff, int soff, v2u v) { __builtin_amdgcn_raw_buffer_store_b64(v, rs, voff, soff, SC1); }

#define LD16C(rs, voff, soff, co) ld16(rs, (voff) + ((co) & 4095), (soff) + ((co) & ~4095))
#define LDWC(rs, voff, soff, co) ldw(rs, (voff) + ((co) & 4095), (soff) + ((co) & ~4095))
DI int opaque(int x) { asm volatile("" : "+s"(x)); return x; }
DI int opaque_v(int x) { asm volatile("" : "+v"(x)); return x; }
#define FRESH() c.lane = opaque_v(c.lane); const int lane = c.lane, t16 = lane & 15, q4 = lane >> 4; (void)t16; (void)q4;

template <int CTRL> DI float dppf(float v) {
  return __builtin_bit_cast(float, __builtin_amdgcn_update_dpp(0, __builtin_bit_cast(int, v), CTRL, 0xf, 0xf, false));
}
DI float row_max(float v) {
  v = fmaxf(v, dppf<0x128>(v)); v = fmaxf(v, dppf<0x124>(v)); v = fmaxf(v, dppf<0x122>(v)); v = fmaxf(v, dppf<0x121>(v));
  return v;
}
DI float row_min(float v) {
  v = fminf(v, dppf<0x128>(v)); v = fminf(v, dppf<0x124>(v)); v = fminf(v, dppf<0x122>(v)); v = fminf(v, dppf<0x121>(v));
  return v;
}
DI float row_sum(float v) {
  v += dppf<0x128>(v); v += dppf<0x124>(v); v += dppf<0x122>(v); v += dppf<0x121>(v);
  return v;
}
DI float x16(float v) { return __shfl_xor(v, 16); }
DI float x32(float v) { return __shfl_xor(v, 32); }
DI float x8(float v) { return dppf<0x128>(v); }

DI float gelu_t(float x) {
  const float u = x * (1.0f + 0.044715f * x * x) * (-2.0f * 0.7978845608028654f);
  return x * __builtin_amdgcn_rcpf(1.0f + __expf(u));
}

DI void poll_ge(gu32 *w, unsigned want, gu32 *tmo) {
  for (unsigned spins = 0;;) {
    if (__hip_atomic_load(w, RLX_AGENT) >= want) return;
    if (++spins > kSpinCap) { __hip_atomic_store(tmo, 1u, RLX_AGENT); return; }
    if ((spins & 1023u) == 0u && __hip_atomic_load(tmo, RLX_AGENT) != 0u) return;
  }
}

template <int NP, int MODE>
DI void ln_pieces(v4u (&a)[NP]) {
  float s = 0.f;
#pragma unroll
  for (int i = 0; i < NP; ++i)
#pragma unroll
    for (int e = 0; e < 4; ++e) s += bflo(a[i][e]) + bfhi(a[i][e]);
  if (MODE == 1) s += x8(s);
  s += x16(s); s += x32(s);
  const float mean = s * (1.0f / D_);
  float q = 0.f;
#pragma unroll
  for (int i = 0; i < NP; ++i)
#pragma unroll
    for (int e = 0; e < 4; ++e) {
      const float d0 = bflo(a[i][e]) - mean, d1 = bfhi(a[i][e]) - mean;
      q += d0 * d0 + d1 * d1;
    }
  if (MODE == 1) q += x8(q);
  q += x16(q); q += x32(q);
  const float mb = rbf(mean);
  const float vb = rbf(q * (1.0f / D_));
  const float sd = rbf(sqrtf(rbf(vb + 1e-5f)));
  const float rs = 1.0f / sd;
#pragma unroll
  for (int i = 0; i < NP; ++i)
#pragma unroll
    for (int e = 0; e < 4; ++e) {
      const unsigned d = pk2(bflo(a[i][e]) - mb, bfhi(a[i][e]) - mb);
      a[i][e] = pk2(bflo(d) * rs, bfhi(d) * rs);
    }
}

constexpr int NP_X = S_ * (D_ / 8);
constexpr int NP_W = (W_END - WQKV_T) / 16;
constexpr int PREP_THREADS = NP_X + NP_W;
constexpr int PREP_BLOCKS = (PREP_THREADS + 255) / 256;

__global__ void __launch_bounds__(256) k_prep(Params p) {
  const int gid = blockIdx.x * 256 + threadIdx.x;
  const __amdgpu_buffer_rsrc_t rs = __builtin_amdgcn_make_buffer_rsrc((void *)p.ws, 0, WS_END, 0x00020000);
  if (blockIdx.x == 0) {
    for (int i = threadIdx.x; i < C_WORDS; i += 256) __hip_atomic_store((gu32 *)p.ws + i, 0u, RLX_AGENT);
  }
  if (gid < NP_X) {
    const int tok = gid / 56, k8 = gid % 56;
    const v4u v = *(const v4u *)(p.x + (size_t)tok * D_ + k8 * 8);
    st16(rs, XIN + (tok >> 4) * TILE_D + k8 * 256 + (tok & 15) * 16, 0, v);
    return;
  }
  const int pid = gid - NP_X;
  if (pid >= NP_W) return;
  const int off = WQKV_T + pid * 16;
  const int lane = pid & 63, r = lane & 15, kg = lane >> 4;
  const unsigned short *src = nullptr;
  int ld = 0, k0 = 0, n = -1;
  bool phi = false;
  int ph_h = 0, ph_f = 0;
  if (off < WO_T) {
    const int fr = (off - WQKV_T) >> 10, h = fr / 84, rem = fr % 84, nt = rem / 14, ks = rem % 14;
    const int part = nt >> 1, c = (nt & 1) * 16 + r;
    src = p.wqkv; ld = 3 * D_; k0 = ks * 32 + kg * 8;
    n = c < DH ? part * D_ + h * DH + c : -1;
  } else if (off < W3_T) {
    const int m = (off - WO_T) / (28 * 14 * 1024);
    const int fr = ((off - WO_T) % (28 * 14 * 1024)) >> 10, nt = fr / 14, ks = fr % 14;
    src = m == 0 ? p.wo : (m == 1 ? p.w1 : p.w2); ld = D_; k0 = ks * 32 + kg * 8; n = nt * 16 + r;
  } else if (off < W4_T) {
    const int fr = (off - W3_T) >> 10, nt = fr / 14, ks = fr % 14;
    src = p.w3; ld = F_; k0 = ks * 32 + kg * 8;
    n = (nt >> 1) * 32 + 8 * (r >> 2) + 4 * (nt & 1) + (r & 3);
  } else if (off < WPHI_T) {
    const int fr = (off - W4_T) >> 10, nt = fr / 56, ks = fr % 56;
    src = p.w4; ld = D_; k0 = ks * 32 + kg * 8; n = nt * 16 + r;
  } else {
    const int fr = (off - WPHI_T) >> 10;
    phi = true; ph_h = fr >> 1; ph_f = (fr & 1) * 16 + r;
  }
  unsigned short e[8];
#pragma unroll
  for (int j = 0; j < 8; ++j) {
    if (phi) {
      const int d = (j < 4) ? 4 * kg + j : 16 + 4 * kg + (j - 4);
      e[j] = (d < DH && ph_f < DH) ? p.wphi[(ph_h * DH + d) * DH + ph_f] : (unsigned short)0;
    } else {
      e[j] = n >= 0 ? src[(size_t)(k0 + j) * ld + n] : (unsigned short)0;
    }
  }
  v4u v;
  v.x = e[0] | ((unsigned)e[1] << 16); v.y = e[2] | ((unsigned)e[3] << 16);
  v.z = e[4] | ((unsigned)e[5] << 16); v.w = e[6] | ((unsigned)e[7] << 16);
  st16(rs, off, 0, v);
}

struct Ctx {
  __amdgpu_buffer_rsrc_t rs;
  gu32 *ctrl;
  LAS unsigned char *L;
  int tid, lane, w;
};

DI void wait1(const Ctx &c, int word, unsigned want) {
  if (c.tid == NTHR - 64) {
    poll_ge(c.ctrl + word, want, c.ctrl + C_TMO);
    __builtin_amdgcn_fence(__ATOMIC_ACQUIRE, "wavefront");
  }
  __syncthreads();
}
DI void wait3(const Ctx &c, int w0, int w1, int w2, unsigned want) {
  if (c.tid == NTHR - 64) {
    poll_ge(c.ctrl + w0, want, c.ctrl + C_TMO);
    poll_ge(c.ctrl + w1, want, c.ctrl + C_TMO);
    poll_ge(c.ctrl + w2, want, c.ctrl + C_TMO);
    __builtin_amdgcn_fence(__ATOMIC_ACQUIRE, "wavefront");
  }
  __syncthreads();
}
DI void publish(const Ctx &c, int word) {
  DRAIN();
  __syncthreads();
  if (c.tid == 0) __hip_atomic_fetch_add(c.ctrl + word, 1u, RLX_AGENT);
}

DI void dma_wqkv(const Params &p, const Ctx &c, int head) {
  const unsigned char *src = p.ws + WQKV_T + head * (84 * 1024) + c.lane * 16;
#pragma unroll
  for (int i = 0; i < 11; ++i) {
    const int fr = c.w + 8 * i;
    if (fr < 84)
      __builtin_amdgcn_global_load_lds((const GAS void *)(src + fr * 1024), (LAS void *)(c.L + L_WQ + fr * 1024), 16, 0, 16);
  }
}

template <int N> DI void load_w(const Ctx &c, bool on, int base, v4u (&w)[28]) {
  base = opaque(base);
  if (on) {
#pragma unroll
    for (int i = 0; i < N; ++i) w[i] = LDWC(c.rs, c.lane * 16, base, i * 1024);
  } else {
#pragma unroll
    for (int i = 0; i < N; ++i) w[i] = v4u{0u, 0u, 0u, 0u};
  }
#pragma unroll
  for (int i = N; i < 28; ++i) w[i] = v4u{0u, 0u, 0u, 0u};
  __builtin_amdgcn_sched_barrier(0);
}
DI void load_w4(const Ctx &c, int j, v4u (&w)[28]) {
  const int base = opaque(W4_T + (4 * j * 56 + 7 * c.w) * 1024);
#pragma unroll
  for (int nt = 0; nt < 4; ++nt)
#pragma unroll
    for (int kk = 0; kk < 7; ++kk) w[nt * 7 + kk] = LDWC(c.rs, c.lane * 16, base, (nt * 56 + kk) * 1024);
  __builtin_amdgcn_sched_barrier(0);
}

template <int LNMODE> DI void stage_a(const Ctx &c, int srcbase, int g, int j) {
  srcbase = opaque(srcbase);
  const int tt = 8 * c.w + (c.lane & 7), kq = c.lane >> 3;
  v4u a[7];
#pragma unroll
  for (int i = 0; i < 7; ++i) {
    if (LNMODE == 3) {
      const int k8 = kq + 8 * i, cg = k8 / 14;
      const int tok = 64 * g + tt + ((cg == 0) ? -1 : (cg == 1) ? 1 : (cg == 2) ? -2 : 2);
      int off = (tok >> 4) * TILE_D + kq * 256 + (tok & 15) * 16;
      if (tok < 0 || tok >= S_) off = 0x7ff00000;
      a[i] = LD16C(c.rs, off, srcbase, i * 2048);
    } else {
      a[i] = LD16C(c.rs, (tt >> 4) * TILE_D + kq * 256 + (tt & 15) * 16, srcbase + 4 * g * TILE_D, i * 2048);
    }
  }
  __builtin_amdgcn_sched_barrier(0);
  if (LNMODE == 1 || LNMODE == 2) ln_pieces<7, 1>(a);
  if (LNMODE == 2) {
#pragma unroll
    for (int i = 0; i < 7; ++i) {
      const int k8 = kq + 8 * i;
      if (i == j) *(LAS v4u *)(c.L + L_Y + (tt * 8 + kq) * 16) = a[i];
      (void)k8;
    }
    ln_pieces<7, 1>(a);
  }
#pragma unroll
  for (int i = 0; i < 7; ++i) {
    const int k8 = kq + 8 * i;
    *(LAS v4u *)(c.L + L_A + (tt >> 4) * TILE_D + k8 * 256 + (tt & 15) * 16) = a[i];
  }
  __syncthreads();
}

DI void gemm64(const Ctx &c, const v4u (&w)[28], f32x4 (&acc)[4]) {
#pragma unroll
  for (int m = 0; m < 4; ++m) acc[m] = f32x4{0.f, 0.f, 0.f, 0.f};
#pragma unroll
  for (int ks = 0; ks < 14; ++ks)
#pragma unroll
    for (int m = 0; m < 4; ++m) {
      const v4u b = *(const LAS v4u *)(c.L + L_A + m * TILE_D + ks * 1024 + c.lane * 16);
      acc[m] = mfma32(w[ks], b, acc[m]);
    }
}

__global__ void __launch_bounds__(NTHR, 2) k_main(Params p) {
  extern __shared__ __attribute__((aligned(16))) unsigned char smem[];
  Ctx c;
  c.rs = __builtin_amdgcn_make_buffer_rsrc((void *)p.ws, 0, WS_END, 0x00020000);
  c.ctrl = (gu32 *)p.ws;
  c.L = (LAS unsigned char *)smem;
  c.tid = threadIdx.x; c.lane = c.tid & 63; c.w = __builtin_amdgcn_readfirstlane(c.tid >> 6);
  const int b = blockIdx.x, xcd = b & 7, r = b >> 3;
  const int g = 4 * xcd + (r >> 3), j = r & 7;
  const int tau = 2 * xcd + (r >> 4), head = r & 15;
  const int w = c.w;

  v4u wr[28];
  dma_wqkv(p, c, head);

  for (int pair = 0; pair < 2; ++pair) {
    const unsigned e1 = pair + 1;
    const int xin = XIN + pair * ACT_D;
    v4u phq_p, phq_n;
    {
      FRESH();
      const v4u wph0 = ldw(c.rs, lane * 16, WPHI_T + (head * 2 + 0) * 1024);
      const v4u wph1 = ldw(c.rs, lane * 16, WPHI_T + (head * 2 + 1) * 1024);
      if (pair > 0) {
        if (c.tid == NTHR - 64) {
          poll_ge(c.ctrl + C_XN + 64 * (2 * tau), 7u * pair, c.ctrl + C_TMO);
          poll_ge(c.ctrl + C_XN + 64 * (2 * tau + 1), 7u * pair, c.ctrl + C_TMO);
          __builtin_amdgcn_fence(__ATOMIC_ACQUIRE, "wavefront");
        }
        __syncthreads();
      }
      v4u xf[14];
      const int xb = opaque(xin + (tau * 8 + w) * TILE_D);
#pragma unroll
      for (int ks = 0; ks < 14; ++ks) xf[ks] = LD16C(c.rs, lane * 16, xb, ks * 1024);
      __builtin_amdgcn_sched_barrier(0);
      ln_pieces<14, 0>(xf);
      DRAIN();
      __syncthreads();
      f32x4 acc[6];
#pragma unroll
      for (int nt = 0; nt < 6; ++nt) acc[nt] = f32x4{0.f, 0.f, 0.f, 0.f};
#pragma unroll
      for (int ks = 0; ks < 14; ++ks)
#pragma unroll
        for (int nt = 0; nt < 6; ++nt) {
          const v4u wf = *(const LAS v4u *)(c.L + L_WQ + (nt * 14 + ks) * 1024 + lane * 16);
          acc[nt] = nt < 4 ? mfma32(wf, xf[ks], acc[nt]) : mfma32(xf[ks], wf, acc[nt]);
        }
      const f32x4 z4 = {0.f, 0.f, 0.f, 0.f};
      {
        v4u qp;
        qp.x = pk2(acc[0][0], acc[0][1]); qp.y = pk2(acc[0][2], acc[0][3]);
        qp.z = pk2(acc[1][0], acc[1][1]); qp.w = pk2(acc[1][2], acc[1][3]);
        f32x4 p0 = mfma32(wph0, qp, z4), p1 = mfma32(wph1, qp, z4);
        const bool v1 = q4 < 3;
        float pv[8];
#pragma unroll
        for (int i = 0; i < 4; ++i) { pv[i] = rbf(p0[i]); pv[4 + i] = rbf(p1[i]); }
        float mx = fmaxf(fmaxf(pv[0], pv[1]), fmaxf(pv[2], pv[3]));
        float mn = fminf(fminf(pv[0], pv[1]), fminf(pv[2], pv[3]));
        const float mx1 = fmaxf(fmaxf(pv[4], pv[5]), fmaxf(pv[6], pv[7]));
        const float mn1 = fminf(fminf(pv[4], pv[5]), fminf(pv[6], pv[7]));
        if (v1) { mx = fmaxf(mx, mx1); mn = fminf(mn, mn1); }
        mx = fmaxf(mx, x16(mx)); mx = fmaxf(mx, x32(mx));
        mn = fminf(mn, x16(mn)); mn = fminf(mn, x32(mn));
        float ep[8], en[8], sp = 0.f, sn = 0.f;
#pragma unroll
        for (int i = 0; i < 8; ++i) {
          const bool ok = i < 4 || v1;
          ep[i] = ok ? __expf(pv[i] - mx) : 0.f;
          en[i] = ok ? __expf(mn - pv[i]) : 0.f;
          sp += ep[i]; sn += en[i];
        }
        sp += x16(sp); sp += x32(sp); sn += x16(sn); sn += x32(sn);
        const float rp = 1.0f / sp, rn = 1.0f / sn;
        phq_p.x = pk2(ep[0] * rp, ep[1] * rp); phq_p.y = pk2(ep[2] * rp, ep[3] * rp);
        phq_p.z = pk2(ep[4] * rp, ep[5] * rp); phq_p.w = pk2(ep[6] * rp, ep[7] * rp);
        phq_n.x = pk2(en[0] * rn, en[1] * rn); phq_n.y = pk2(en[2] * rn, en[3] * rn);
        phq_n.z = pk2(en[4] * rn, en[5] * rn); phq_n.w = pk2(en[6] * rn, en[7] * rn);
      }
      {
        v4u kp;
        kp.x = pk2(acc[2][0], acc[2][1]); kp.y = pk2(acc[2][2], acc[2][3]);
        kp.z = pk2(acc[3][0], acc[3][1]); kp.w = pk2(acc[3][2], acc[3][3]);
        f32x4 p0 = mfma32(kp, wph0, z4), p1 = mfma32(kp, wph1, z4);
        const bool v1 = t16 < 12;
        float fp0[4], fp1[4], fn0[4], fn1[4];
#pragma unroll
        for (int i = 0; i < 4; ++i) {
          const float a0 = rbf(p0[i]), a1 = rbf(p1[i]);
          const float mx = row_max(v1 ? fmaxf(a0, a1) : a0);
          const float mn = row_min(v1 ? fminf(a0, a1) : a0);
          const float e0 = __expf(a0 - mx), e1_ = v1 ? __expf(a1 - mx) : 0.f;
          const float n0 = __expf(mn - a0), n1 = v1 ? __expf(mn - a1) : 0.f;
          const float rp = 1.0f / row_sum(e0 + e1_), rn = 1.0f / row_sum(n0 + n1);
          fp0[i] = e0 * rp; fp1[i] = e1_ * rp; fn0[i] = n0 * rn; fn1[i] = n1 * rn;
        }
        LAS v2u *ex = (LAS v2u *)(c.L + L_EX + w * 3072) + lane;
        ex[0 * 64] = v2u{pk2(fp0[0], fp0[1]), pk2(fp0[2], fp0[3])};
        ex[1 * 64] = v2u{pk2(fp1[0], fp1[1]), pk2(fp1[2], fp1[3])};
        ex[2 * 64] = v2u{pk2(fn0[0], fn0[1]), pk2(fn0[2], fn0[3])};
        ex[3 * 64] = v2u{pk2(fn1[0], fn1[1]), pk2(fn1[2], fn1[3])};
        ex[4 * 64] = v2u{pk2(acc[4][0], acc[4][1]), pk2(acc[4][2], acc[4][3])};
        v2u vv = v2u{pk2(acc[5][0], acc[5][1]), pk2(acc[5][2], acc[5][3])};
        if (t16 == 12) vv = v2u{0x3f803f80u, 0x3f803f80u};
        ex[5 * 64] = vv;
      }
      __syncthreads();
      {
        const int ft = w >> 1, dt = w & 1;
        f32x4 kv = z4;
#pragma unroll
        for (int m = 0; m < 8; ++m) {
          const v2u a = *((const LAS v2u *)(c.L + L_EX + m * 3072) + ft * 64 + lane);
          const v2u bb = *((const LAS v2u *)(c.L + L_EX + m * 3072) + (4 + dt) * 64 + lane);
          kv = mfma16(a, bb, kv);
        }
        st16(c.rs, lane * 16, KVP_B + ((pair * 16 + head) * 16 + tau) * 8192 + w * 1024, __builtin_bit_cast(v4u, kv));
      }
      publish(c, C_KV + 64 * head);
    }
    {
      FRESH();
      load_w<14>(c, j < 7 && w < 4, WO_T + (4 * j + w) * 14 * 1024, wr);
      wait1(c, C_KV + 64 * head, 16u * e1);
      v4u pr[16];
      const int kb = opaque(KVP_B + (pair * 16 + head) * 16 * 8192 + w * 1024);
#pragma unroll
      for (int t = 0; t < 16; ++t) pr[t] = ld16(c.rs, lane * 16, kb + t * 8192);
      __builtin_amdgcn_sched_barrier(0);
      f32x4 s = __builtin_bit_cast(f32x4, pr[0]);
#pragma unroll
      for (int t = 1; t < 16; ++t) s += __builtin_bit_cast(f32x4, pr[t]);
      {
        const int ft = w >> 1, dt = w & 1, d = 16 * dt + t16, part = ft >> 1, half = ft & 1;
        *(LAS v2u *)(c.L + L_KVS + ((d * 2 + part) * 4 + q4) * 16 + half * 8) = v2u{pk2(s[0], s[1]), pk2(s[2], s[3])};
      }
      __syncthreads();
      f32x4 nm[2];
#pragma unroll
      for (int dt = 0; dt < 2; ++dt) {
        const v4u ap = *(const LAS v4u *)(c.L + L_KVS + (((16 * dt + t16) * 2 + 0) * 4 + q4) * 16);
        const v4u an = *(const LAS v4u *)(c.L + L_KVS + (((16 * dt + t16) * 2 + 1) * 4 + q4) * 16);
        nm[dt] = mfma32(ap, phq_p, f32x4{0.f, 0.f, 0.f, 0.f});
        nm[dt] = mfma32(an, phq_n, nm[dt]);
      }
      const float den0 = __shfl(nm[1][0], 48 + t16);
      const float rden = 1.0f / rbf(rbf(den0) + 1e-6f);
#pragma unroll
      for (int dt = 0; dt < 2; ++dt) {
        if (dt == 0 || q4 < 3) {
          const int col = DH * head + 16 * dt + 4 * q4;
          v2u o;
          o.x = pk2(rbf(nm[dt][0]) * rden, rbf(nm[dt][1]) * rden);
          o.y = pk2(rbf(nm[dt][2]) * rden, rbf(nm[dt][3]) * rden);
          st8(c.rs, (col >> 3) * 256 + t16 * 16 + ((col >> 2) & 1) * 8, O_B + pair * ACT_D + (tau * 8 + w) * TILE_D, o);
        }
      }
      publish(c, C_O + 64 * tau);
    }
    if (j < 7) {
#define EOFF() const int eoff = ((64 * j + 16 * w + 4 * q4) >> 3) * 256 + t16 * 16 + (q4 & 1) * 8
      {
        FRESH(); EOFF();
        v2u xres[4];
        if (w < 4) {
#pragma unroll
          for (int m = 0; m < 4; ++m) xres[m] = ld8(c.rs, eoff, xin + (4 * g + m) * TILE_D);
        }
        wait1(c, C_O + 64 * (g >> 1), 16u * e1);
        stage_a<0>(c, O_B + pair * ACT_D, g, j);
        if (w < 4) {
          f32x4 acc[4];
          gemm64(c, wr, acc);
#pragma unroll
          for (int m = 0; m < 4; ++m) {
            v2u o;
            o.x = pk2(bflo(xres[m].x) + rbf(acc[m][0]), bfhi(xres[m].x) + rbf(acc[m][1]));
            o.y = pk2(bflo(xres[m].y) + rbf(acc[m][2]), bfhi(xres[m].y) + rbf(acc[m][3]));
            st8(c.rs, eoff, XP_B + pair * ACT_D + (4 * g + m) * TILE_D, o);
          }
        }
        publish(c, C_XP + 64 * g);
      }
      {
        FRESH(); EOFF();
        load_w<14>(c, w < 4, W1_T + (4 * j + w) * 14 * 1024, wr);
        wait1(c, C_XP + 64 * g, 7u * e1);
        stage_a<2>(c, XP_B + pair * ACT_D, g, j);
        if (w < 4) {
          f32x4 acc[4];
          gemm64(c, wr, acc);
#pragma unroll
          for (int m = 0; m < 4; ++m) {
            v2u o;
            o.x = pk2(gelu_t(rbf(acc[m][0])), gelu_t(rbf(acc[m][1])));
            o.y = pk2(gelu_t(rbf(acc[m][2])), gelu_t(rbf(acc[m][3])));
            st8(c.rs, eoff, H_B + pair * ACT_D + (4 * g + m) * TILE_D, o);
          }
        }
        publish(c, C_H + 64 * g);
      }
      {
        FRESH(); EOFF();
        load_w<14>(c, w < 4, W2_T + (4 * j + w) * 14 * 1024, wr);
        wait3(c, C_H + 64 * (g > 0 ? g - 1 : g), C_H + 64 * g, C_H + 64 * (g < 31 ? g + 1 : g), 7u * e1);
        stage_a<3>(c, H_B + pair * ACT_D, g, j);
        if (w < 4) {
          f32x4 acc[4];
          gemm64(c, wr, acc);
#pragma unroll
          for (int m = 0; m < 4; ++m) {
            const v2u y = *(const LAS v2u *)(c.L + L_Y + ((16 * m + t16) * 8 + 2 * w + (q4 >> 1)) * 16 + (q4 & 1) * 8);
            v2u o;
            o.x = pk2(bflo(y.x) + rbf(acc[m][0]), bfhi(y.x) + rbf(acc[m][1]));
            o.y = pk2(bflo(y.y) + rbf(acc[m][2]), bfhi(y.y) + rbf(acc[m][3]));
            st8(c.rs, eoff, X1_B + pair * ACT_D + (4 * g + m) * TILE_D, o);
          }
        }
        publish(c, C_X1 + 64 * g);
      }
      {
        FRESH();
        load_w<28>(c, true, W3_T + (16 * j + 2 * w) * 14 * 1024, wr);
        wait1(c, C_X1 + 64 * g, 7u * e1);
        stage_a<1>(c, X1_B + pair * ACT_D, g, j);
        f32x4 acc[4][2];
#pragma unroll
        for (int m = 0; m < 4; ++m) { acc[m][0] = f32x4{0.f, 0.f, 0.f, 0.f}; acc[m][1] = f32x4{0.f, 0.f, 0.f, 0.f}; }
#pragma unroll
        for (int ks = 0; ks < 14; ++ks)
#pragma unroll
          for (int m = 0; m < 4; ++m) {
            const v4u bb = *(const LAS v4u *)(c.L + L_A + m * TILE_D + ks * 1024 + lane * 16);
            acc[m][0] = mfma32(wr[ks], bb, acc[m][0]);
            acc[m][1] = mfma32(wr[14 + ks], bb, acc[m][1]);
          }
#pragma unroll
        for (int m = 0; m < 4; ++m) {
          v4u o;
          o.x = pk2(gelu_t(rbf(acc[m][0][0])), gelu_t(rbf(acc[m][0][1])));
          o.y = pk2(gelu_t(rbf(acc[m][0][2])), gelu_t(rbf(acc[m][0][3])));
          o.z = pk2(gelu_t(rbf(acc[m][1][0])), gelu_t(rbf(acc[m][1][1])));
          o.w = pk2(gelu_t(rbf(acc[m][1][2])), gelu_t(rbf(acc[m][1][3])));
          st16(c.rs, lane * 16, G_B + pair * ACT_F + (4 * g + m) * TILE_F + (32 * j + 4 * w) * 256, o);
        }
        publish(c, C_G + 64 * g);
      }
      {
        FRESH();
        load_w4(c, j, wr);
        const int roff = q4 * TILE_D + t16 * 16;
        const int rso = 4 * g * TILE_D + (8 * j + w) * 256;
        const v4u rxp = ld16(c.rs, roff, XP_B + pair * ACT_D + rso);
        const v4u rx1 = ld16(c.rs, roff, X1_B + pair * ACT_D + rso);
        wait1(c, C_G + 64 * g, 7u * e1);
        const int abase = opaque(G_B + pair * ACT_F + 4 * g * TILE_F + 7 * w * 1024);
        constexpr int RING = 10;
        v4u a[RING];
#pragma unroll
        for (int i = 0; i < RING; ++i) a[i] = LD16C(c.rs, lane * 16, abase, (i / 7) * TILE_F + (i % 7) * 1024);
        __builtin_amdgcn_sched_barrier(0);
#pragma unroll
        for (int m = 0; m < 4; ++m) {
          f32x4 acc[4];
#pragma unroll
          for (int nt = 0; nt < 4; ++nt) acc[nt] = f32x4{0.f, 0.f, 0.f, 0.f};
#pragma unroll
          for (int kk = 0; kk < 7; ++kk) {
            const int i = m * 7 + kk;
            const v4u cur = a[i % RING];
            if (i + RING < 28) a[i % RING] = LD16C(c.rs, lane * 16, abase, ((i + RING) / 7) * TILE_F + ((i + RING) % 7) * 1024);
#pragma unroll
            for (int nt = 0; nt < 4; ++nt) acc[nt] = mfma32(wr[nt * 7 + kk], cur, acc[nt]);
            __builtin_amdgcn_sched_barrier(0);
          }
#pragma unroll
          for (int nt = 0; nt < 4; ++nt) *(LAS f32x4 *)(c.L + L_RED + ((w * 16 + m * 4 + nt) * 64 + lane) * 16) = acc[nt];
        }
        __syncthreads();
        f32x4 s0 = {0.f, 0.f, 0.f, 0.f}, s1 = {0.f, 0.f, 0.f, 0.f};
        {
          const int nt = w >> 1, qq = 2 * (w & 1);
#pragma unroll
          for (int ww = 0; ww < 8; ++ww) {
            s0 += *(const LAS f32x4 *)(c.L + L_RED + ((ww * 16 + q4 * 4 + nt) * 64 + t16 + 16 * qq) * 16);
            s1 += *(const LAS f32x4 *)(c.L + L_RED + ((ww * 16 + q4 * 4 + nt) * 64 + t16 + 16 * (qq + 1)) * 16);
          }
        }
        v4u o;
        o.x = pk2(bflo(rxp.x) + rbf(bflo(rx1.x) + rbf(s0[0])), bfhi(rxp.x) + rbf(bfhi(rx1.x) + rbf(s0[1])));
        o.y = pk2(bflo(rxp.y) + rbf(bflo(rx1.y) + rbf(s0[2])), bfhi(rxp.y) + rbf(bfhi(rx1.y) + rbf(s0[3])));
        o.z = pk2(bflo(rxp.z) + rbf(bflo(rx1.z) + rbf(s1[0])), bfhi(rxp.z) + rbf(bfhi(rx1.z) + rbf(s1[1])));
        o.w = pk2(bflo(rxp.w) + rbf(bflo(rx1.w) + rbf(s1[2])), bfhi(rxp.w) + rbf(bfhi(rx1.w) + rbf(s1[3])));
        if (pair == 0) {
          st16(c.rs, roff, XIN + ACT_D + rso, o);
          publish(c, C_XN + 64 * g);
        } else {
          *(v4u *)(p.out + (size_t)(64 * g + 16 * q4 + t16) * D_ + 64 * j + 8 * w) = o;
        }
      }
    }
    if (pair == 0) {
      __syncthreads();
      c.lane = opaque_v(c.lane);
      dma_wqkv(p, c, head);
    }
  }
}

}

extern "C" void kernel_launch(void *const *d_in, const int *in_sizes, int n_in, void *d_out, int out_size, void *d_ws,
                              size_t ws_size, hipStream_t stream) {
  Params p;
  p.x = (const unsigned short *)d_in[0];
  p.wqkv = (const unsigned short *)d_in[1];
  p.wphi = (const unsigned short *)d_in[2];
  p.wo = (const unsigned short *)d_in[3];
  p.w1 = (const unsigned short *)d_in[5];
  p.w2 = (const unsigned short *)d_in[6];
  p.w3 = (const unsigned short *)d_in[7];
  p.w4 = (const unsigned short *)d_in[8];
  p.out = (unsigned short *)d_out;
  p.ws = (unsigned char *)d_ws;
  (void)hipFuncSetAttribute((const void *)k_main, hipFuncAttributeMaxDynamicSharedMemorySize, LDS_BYTES);
  hipLaunchKernelGGL(k_prep, dim3(PREP_BLOCKS), dim3(256), 0, stream, p);
  hipLaunchKernelGGL(k_main, dim3(256), dim3(NTHR), LDS_BYTES, stream, p);
}
```

```cpp
#include <hip/hip_runtime.h>
#include <hip/hip_bf16.h>
#include <stdint.h>

#define GAS __attribute__((address_space(1)))
#define LAS __attribute__((address_space(3)))
#define RLX_AGENT __ATOMIC_RELAXED, __HIP_MEMORY_SCOPE_AGENT
#define DI __device__ __forceinline__
typedef unsigned v4u __attribute__((ext_vector_type(4)));
typedef unsigned v2u __attribute__((ext_vector_type(2)));
typedef float f32x4 __attribute__((ext_vector_type(4)));
typedef float f32x2 __attribute__((ext_vector_type(2)));
typedef short bf16x8 __attribute__((ext_vector_type(8)));
typedef short bf16x4 __attribute__((ext_vector_type(4)));
typedef __bf16 bfv2 __attribute__((ext_vector_type(2)));
typedef GAS unsigned gu32;

namespace {

constexpr int S_ = 2048, D_ = 448, DH = 28, F_ = 1792;
constexpr int NTHR = 512;
constexpr int TILE_D = (D_ / 8) * 256;
constexpr int TILE_F = (F_ / 8) * 256;
constexpr int ACT_D = 128 * TILE_D;
constexpr int ACT_F = 128 * TILE_F;

#define CSTR_W 64
#define POLL_SLEEP 1
constexpr int CSTR = CSTR_W;
constexpr int C_TMO = 0;
constexpr int C_XN = 1;
constexpr int C_KV = C_XN + 32;
constexpr int C_O = C_KV + 16;
constexpr int C_XP = C_O + 16;
constexpr int C_H = C_XP + 16;
constexpr int C_X1 = C_H + 16;
constexpr int C_G = C_X1 + 16;
constexpr int C_NCTR = C_G + 32;
constexpr int CTRL_BYTES = ((C_NCTR * CSTR * 4 + 65535) / 65536) * 65536;
static_assert(C_NCTR * CSTR * 4 <= CTRL_BYTES, "control block");
constexpr int WQKV_T = CTRL_BYTES;
constexpr int WO_T = WQKV_T + 96 * 14 * 1024;
constexpr int W1_T = WO_T + 28 * 14 * 1024;
constexpr int W2_T = W1_T + 28 * 14 * 1024;
constexpr int W3_T = W2_T + 28 * 14 * 1024;
constexpr int W4_T = W3_T + 112 * 14 * 1024;
constexpr int WPHI_T = W4_T + 28 * 56 * 1024;
constexpr int CS_QKV = WPHI_T + 16 * 2 * 1024;
constexpr int CS_W1 = CS_QKV + 96 * 16 * 4;
constexpr int CS_W3 = CS_W1 + 448 * 4;
constexpr int W_END = CS_W3 + 1792 * 4;
constexpr int XIN = (W_END + 4095) & ~4095;
constexpr int O_B = XIN + 2 * ACT_D;
constexpr int XP_B = O_B + 2 * ACT_D;
constexpr int H_B = XP_B + 2 * ACT_D;
constexpr int X1_B = H_B + 2 * ACT_D;
constexpr int G_B = X1_B + 2 * ACT_D;
constexpr int KVP_B = G_B + 2 * ACT_F;
constexpr int WS_END = KVP_B + 2 * 16 * 16 * 8192;

constexpr int L_A = 0;
constexpr int L_ST = 57344;
constexpr int L_RED = 0;
constexpr int L_WQ = 65536;
constexpr int PK_STR = 136, V_STR = 72;
constexpr int L_PK = 0;
constexpr int L_V = 17408;
constexpr int L_KVS = 26624;
constexpr int L_WO = 32768;
constexpr int L_W1 = 65536;
constexpr int L_W2 = 94208;
constexpr int LDS_BYTES = 151552;

constexpr unsigned kSpinCap = 1u << 22;
#define STAMP(i) do { } while (0)

struct Params {
  const unsigned short *x, *wqkv, *wphi, *wo, *w1, *w2, *w3, *w4;
  unsigned short *out;
  unsigned char *ws;
};

DI float asf(unsigned u) { return __builtin_bit_cast(float, u); }
DI unsigned asu(float f) { return __builtin_bit_cast(unsigned, f); }
DI float bflo(unsigned u) { return asf(u << 16); }
DI float bfhi(unsigned u) { return asf(u & 0xffff0000u); }
DI unsigned pk2(float a, float b) {
  f32x2 v = {a, b};
  return __builtin_bit_cast(unsigned, __builtin_convertvector(v, bfv2));
}
DI float rbf(float a) { return (float)(__bf16)a; }
DI float rcp(float a) { return __builtin_amdgcn_rcpf(a); }
DI float ex2(float a) { return __builtin_amdgcn_exp2f(a); }
DI float dot2(unsigned a, unsigned b, float c) {
  return __builtin_amdgcn_fdot2_f32_bf16(__builtin_bit_cast(bfv2, a), __builtin_bit_cast(bfv2, b), c, false);
}
DI f32x4 mfma32(v4u a, v4u b, f32x4 c) {
  return __builtin_amdgcn_mfma_f32_16x16x32_bf16(__builtin_bit_cast(bf16x8, a), __builtin_bit_cast(bf16x8, b), c, 0, 0, 0);
}
#define DRAIN() asm volatile("s_waitcnt vmcnt(0)" ::: "memory")
constexpr int SC1 = 16;
DI v4u ld16(__amdgpu_buffer_rsrc_t rs, int voff, int soff) { return __builtin_amdgcn_raw_buffer_load_b128(rs, voff, soff, SC1); }
DI v2u ld8(__amdgpu_buffer_rsrc_t rs, int voff, int soff) { return __builtin_amdgcn_raw_buffer_load_b64(rs, voff, soff, SC1); }
DI void st16(__amdgpu_buffer_rsrc_t rs, int voff, int soff, v4u v) { __builtin_amdgcn_raw_buffer_store_b128(v, rs, voff + soff, 0, SC1); }
DI void st8(__amdgpu_buffer_rsrc_t rs, int voff, int soff, v2u v) { __builtin_amdgcn_raw_buffer_store_b64(v, rs, voff, soff, SC1); }
DI void st4(__amdgpu_buffer_rsrc_t rs, int voff, int soff, unsigned v) { __builtin_amdgcn_raw_buffer_store_b32(v, rs, voff, soff, SC1); }
#define LD16C(rs, voff, soff, co) ld16(rs, (voff) + ((co) & 4095), (soff) + ((co) & ~4095))
DI int opaque(int x) { asm volatile("" : "+s"(x)); return x; }
DI int opaque_v(int x) { asm volatile("" : "+v"(x)); return x; }
#define FRESH() c.lane = opaque_v(c.lane); const int lane = c.lane, t16 = lane & 15, q4 = lane >> 4; (void)t16; (void)q4;
#define SB() __builtin_amdgcn_sched_barrier(0)

template <int CTRL> DI float dppf(float v) {
  return asf(__builtin_amdgcn_update_dpp(0, (int)asu(v), CTRL, 0xf, 0xf, false));
}
DI float sum_q(float v) {
  auto r = __builtin_amdgcn_permlane16_swap(asu(v), asu(v), false, false);
  v = asf(r[0]) + asf(r[1]);
  auto s = __builtin_amdgcn_permlane32_swap(asu(v), asu(v), false, false);
  return asf(s[0]) + asf(s[1]);
}
DI float max_q(float v) {
  auto r = __builtin_amdgcn_permlane16_swap(asu(v), asu(v), false, false);
  v = fmaxf(asf(r[0]), asf(r[1]));
  auto s = __builtin_amdgcn_permlane32_swap(asu(v), asu(v), false, false);
  return fmaxf(asf(s[0]), asf(s[1]));
}
DI float min_q(float v) {
  auto r = __builtin_amdgcn_permlane16_swap(asu(v), asu(v), false, false);
  v = fminf(asf(r[0]), asf(r[1]));
  auto s = __builtin_amdgcn_permlane32_swap(asu(v), asu(v), false, false);
  return fminf(asf(s[0]), asf(s[1]));
}

constexpr float LOG2E = 1.4426950408889634f;
DI unsigned gelu2(float a, float b) {
  const f32x2 x = {a, b};
  constexpr float c0 = -2.0f * 0.7978845608028654f * LOG2E, c1 = c0 * 0.044715f;
  const f32x2 u = x * (x * x * c1 + c0);
  f32x2 e = {ex2(u.x), ex2(u.y)};
  e = e + 1.0f;
  const f32x2 r = {rcp(e.x), rcp(e.y)};
  const f32x2 o = x * r;
  return pk2(o.x, o.y);
}

DI void poll_ge(gu32 *w, unsigned want, gu32 *tmo) {
#pragma nounroll
  for (unsigned spins = 0;;) {
    if (__hip_atomic_load(w, RLX_AGENT) >= want) return;
    __builtin_amdgcn_s_sleep(POLL_SLEEP);
    if (++spins > kSpinCap) { __hip_atomic_store(tmo, 1u, RLX_AGENT); return; }
    if ((spins & 1023u) == 0u && __hip_atomic_load(tmo, RLX_AGENT) != 0u) return;
  }
}

struct LnC { float A, B, mb, r; };
template <bool TWICE> DI LnC ln_coef(float s1, float s2) {
  const float mean = s1 * (1.0f / D_);
  const float var = fmaxf(s2 * (1.0f / D_) - mean * mean, 0.f);
  LnC o;
  o.mb = rbf(mean);
  const float sd = rbf(__builtin_amdgcn_sqrtf(rbf(rbf(var) + 1e-5f)));
  o.r = rcp(sd);
  if (TWICE) {
    const float my = (mean - o.mb) * o.r, vy = var * o.r * o.r;
    const float mby = rbf(my);
    const float ry = rcp(rbf(__builtin_amdgcn_sqrtf(rbf(rbf(vy) + 1e-5f))));
    o.A = o.r * ry;
    o.B = (o.mb * o.r + mby) * ry;
  } else {
    o.A = o.r;
    o.B = o.mb * o.r;
  }
  return o;
}

constexpr int NB_X = S_ * (D_ / 8) / 256;
constexpr int NJ_Q = 3 * 4 * 14;
constexpr int NJ_S = 3 * 7 * 14, NJ_3 = 28 * 14, NJ_4 = 7 * 56;
constexpr int NB_W = NJ_Q + NJ_S + NJ_3 + NJ_4;
constexpr int NB_PHI = 8;
constexpr int NB_CS = 21 + 7 + 28;
constexpr int PREP_BLOCKS = NB_W + NB_CS + NB_PHI + NB_X;

__global__ void __launch_bounds__(256) k_prep(Params p) {
  __shared__ __attribute__((aligned(16))) unsigned char sm[32 * 240];
  f32x4 *red = (f32x4 *)sm;
  const __amdgpu_buffer_rsrc_t rs = __builtin_amdgcn_make_buffer_rsrc((void *)p.ws, 0, WS_END, 0x00020000);
  const int tid = threadIdx.x;
  int b = blockIdx.x;
  if (b < NJ_Q) {
    const int ks = b % 14, hg = (b / 14) & 3, part = b / 56;
    const unsigned short *src = p.wqkv + (size_t)(ks * 32) * (3 * D_) + part * D_ + hg * 112;
#pragma unroll
    for (int i = 0; i < 2; ++i) {
      const int ch = tid + 256 * i;
      if (ch < 448) {
        const int row = ch / 14, cc = ch % 14;
        *(v4u *)(sm + row * 240 + cc * 16) = *(const v4u *)(src + (size_t)row * (3 * D_) + cc * 8);
      }
    }
    __syncthreads();
#pragma unroll
    for (int i = 0; i < 2; ++i) {
      const int f = (tid >> 6) + 4 * i, slot = tid & 63, r = slot & 15, kg = slot >> 4;
      const int cl = 16 * (f & 1) + r;
      const bool valid = cl < DH;
      const unsigned short *q_ = (const unsigned short *)(sm + (8 * kg) * 240) + 28 * (f >> 1) + (valid ? cl : 0);
      v4u o;
#pragma unroll
      for (int jj = 0; jj < 4; ++jj) o[jj] = valid ? (unsigned)q_[(2 * jj) * 120] | ((unsigned)q_[(2 * jj + 1) * 120] << 16) : 0u;
      const int nt = (4 * hg + (f >> 1)) * 6 + part * 2 + (f & 1);
      st16(rs, WQKV_T + (nt * 14 + ks) * 1024 + slot * 16, 0, o);
    }
    return;
  }
  if (b < NB_W) {
    int jb = b - NJ_Q;
    const unsigned short *src; int ld, dst, cb, ks, KS; bool perm = true;
    if (jb < NJ_S) {
      const int m = jb / 98, rr = jb % 98;
      cb = rr / 14; ks = rr % 14; KS = 14;
      src = m == 0 ? p.wo : (m == 1 ? p.w1 : p.w2); ld = D_; dst = WO_T + m * (28 * 14 * 1024);
    } else if (jb < NJ_S + NJ_3) {
      jb -= NJ_S; cb = jb / 14; ks = jb % 14; KS = 14; src = p.w3; ld = F_; dst = W3_T;
    } else {
      jb -= NJ_S + NJ_3; cb = jb / 56; ks = jb % 56; KS = 56; src = p.w4; ld = D_; dst = W4_T; perm = false;
    }
    {
      const int row = tid >> 3, cc = tid & 7;
      *(v4u *)(sm + row * 144 + cc * 16) = *(const v4u *)(src + (size_t)(ks * 32 + row) * ld + cb * 64 + cc * 8);
    }
    __syncthreads();
    const int f = tid >> 6, slot = tid & 63, r = slot & 15, kg = slot >> 4;
    const int cl = perm ? 32 * (f >> 1) + 8 * (r >> 2) + 4 * (f & 1) + (r & 3) : 16 * f + r;
    const unsigned short *q_ = (const unsigned short *)(sm + (8 * kg) * 144) + cl;
    v4u o;
#pragma unroll
    for (int jj = 0; jj < 4; ++jj) o[jj] = (unsigned)q_[(2 * jj) * 72] | ((unsigned)q_[(2 * jj + 1) * 72] << 16);
    st16(rs, dst + ((4 * cb + f) * KS + ks) * 1024 + slot * 16, 0, o);
    return;
  }
  b -= NB_W;
  if (b < NB_CS) {
    const unsigned short *src; int ld, col0;
    if (b < 21) { src = p.wqkv; ld = 3 * D_; col0 = b * 64; }
    else if (b < 28) { src = p.w1; ld = D_; col0 = (b - 21) * 64; }
    else { src = p.w3; ld = F_; col0 = (b - 28) * 64; }
    const int n4 = tid & 15, ksl = tid >> 4;
    const unsigned short *s0 = src + (size_t)(ksl * 28) * ld + col0 + 4 * n4;
    f32x4 s = {0.f, 0.f, 0.f, 0.f};
#pragma unroll 7
    for (int k = 0; k < 28; ++k) {
      const v2u e = *(const v2u *)(s0 + (size_t)k * ld);
      s += f32x4{bflo(e.x), bfhi(e.x), bflo(e.y), bfhi(e.y)};
    }
    red[tid] = s;
    __syncthreads();
    if (tid < 64) {
      float a = 0.f;
#pragma unroll
      for (int k = 0; k < 16; ++k) a += ((const float *)red)[(k * 16 + (tid >> 2)) * 4 + (tid & 3)];
      const int col = col0 + tid;
      int off;
      if (b < 21) {
        const int part = col / D_, rem = col % D_, head = rem / DH, cc = rem % DH;
        off = CS_QKV + ((head * 6 + part * 2 + (cc >> 4)) * 16 + (cc & 15)) * 4;
      } else if (b < 28) off = CS_W1 + col * 4;
      else off = CS_W3 + col * 4;
      st4(rs, off, 0, asu(a));
    } else if (b == 0 && tid >= 64) {
      const int i = tid - 64;
      if (i < 192) st4(rs, CS_QKV + (((i >> 2) * 2 + 1) * 16 + 12 + (i & 3)) * 4, 0, 0u);
    }
    return;
  }
  b -= NB_CS;
  if (b < NB_PHI) {
    const int pid = b * 256 + tid, fr = pid >> 6, lane = pid & 63, r = lane & 15, kg = lane >> 4;
    const int h = fr >> 1, f = (fr & 1) * 16 + r;
    unsigned short e[8];
#pragma unroll
    for (int j = 0; j < 8; ++j) {
      const int d = (j < 4) ? 4 * kg + j : 16 + 4 * kg + (j - 4);
      e[j] = (d < DH && f < DH) ? p.wphi[(h * DH + d) * DH + f] : (unsigned short)0;
    }
    v4u v;
    v.x = e[0] | ((unsigned)e[1] << 16); v.y = e[2] | ((unsigned)e[3] << 16);
    v.z = e[4] | ((unsigned)e[5] << 16); v.w = e[6] | ((unsigned)e[7] << 16);
    st16(rs, WPHI_T + pid * 16, 0, v);
    if (pid < C_NCTR) __hip_atomic_store((gu32 *)p.ws + CSTR * pid, 0u, RLX_AGENT);
    return;
  }
  b -= NB_PHI;
  {
    const int fr = b * 4 + (tid >> 6), tile = fr / 14, ks = fr % 14, lane = tid & 63;
    const v4u v = *(const v4u *)(p.x + (size_t)(tile * 16 + (lane & 15)) * D_ + ks * 32 + (lane >> 4) * 8);
    st16(rs, XIN + tile * TILE_D + ks * 1024 + lane * 16, 0, v);
  }
}

struct Ctx {
  __amdgpu_buffer_rsrc_t rs;
  gu32 *ctrl;
  LAS unsigned char *L;
  int tid, lane, w;
};

DI void wait1(const Ctx &c, int ctr, unsigned want) {
  if (c.tid == NTHR - 64) {
    poll_ge(c.ctrl + CSTR * ctr, want, c.ctrl);
    __builtin_amdgcn_fence(__ATOMIC_ACQUIRE, "wavefront");
  }
  __syncthreads();
}
DI void wait2(const Ctx &c, int c0, int c1, unsigned want) {
  if (c.tid == NTHR - 64) {
    poll_ge(c.ctrl + CSTR * c0, want, c.ctrl);
    poll_ge(c.ctrl + CSTR * c1, want, c.ctrl);
    __builtin_amdgcn_fence(__ATOMIC_ACQUIRE, "wavefront");
  }
  __syncthreads();
}
DI void wait3(const Ctx &c, int c0, int c1, int c2, unsigned want) {
  if (c.tid == NTHR - 64) {
    poll_ge(c.ctrl + CSTR * c0, want, c.ctrl);
    poll_ge(c.ctrl + CSTR * c1, want, c.ctrl);
    poll_ge(c.ctrl + CSTR * c2, want, c.ctrl);
    __builtin_amdgcn_fence(__ATOMIC_ACQUIRE, "wavefront");
  }
  __syncthreads();
}
DI void publish(const Ctx &c, int ctr) {
  DRAIN();
  __syncthreads();
  if (c.tid == 0) __hip_atomic_fetch_add(c.ctrl + CSTR * ctr, 1u, RLX_AGENT);
}

DI void dma1k(const unsigned char *gsrc, unsigned lds_dst) {
  unsigned keep;
  asm volatile("s_mov_b32 %0, m0\n\ts_mov_b32 m0, %2\n\ts_nop 0\n\tglobal_load_lds_dwordx4 %1, off sc1\n\ts_mov_b32 m0, %0"
               : "=&s"(keep) : "v"(gsrc), "s"(lds_dst) : "memory");
}
template <int NFR> DI void dma_run(const Params &p, const Ctx &c, int src, int dst) {
  const unsigned char *g = p.ws + src + c.w * 1024 + c.lane * 16;
  const unsigned l = (unsigned)(size_t)c.L + dst + c.w * 1024;
#pragma unroll
  for (int i = 0; i < (NFR + 7) / 8; ++i) {
    if (c.w + 8 * i < NFR) dma1k(g + i * 8192, l + i * 8192);
  }
}

template <int LO, int HI, int N> DI void load_run(const Ctx &c, int base, v4u (&w)[N]) {
  base = opaque(base);
#pragma unroll
  for (int i = LO; i < HI; ++i) w[i] = LD16C(c.rs, c.lane * 16, base, i * 1024);
  SB();
}

template <int MODE> DI void stage_issue(const Ctx &c, int srcbase, int g, v4u (&a)[7]) {
  srcbase = opaque(srcbase);
  const int tt = 8 * c.w + (c.lane & 7), kq = c.lane >> 3;
#pragma unroll
  for (int i = 0; i < 7; ++i) {
    if (MODE == 3) {
      const int k8 = kq + 8 * i, cg = k8 / 14;
      const int tok = 64 * g + tt + ((cg == 0) ? -1 : (cg == 1) ? 1 : (cg == 2) ? -2 : 2);
      int off = (tok >> 4) * TILE_D + kq * 256 + (tok & 15) * 16;
      if (tok < 0 || tok >= S_) off = 0x7ff00000;
      a[i] = LD16C(c.rs, off, srcbase, i * 2048);
    } else {
      a[i] = LD16C(c.rs, (tt >> 4) * TILE_D + kq * 256 + (tt & 15) * 16, srcbase + 4 * g * TILE_D, i * 2048);
    }
  }
  SB();
}
template <int MODE> DI void stage_finish(const Ctx &c, int j, v4u (&a)[7]) {
  const int tt = 8 * c.w + (c.lane & 7), kq = c.lane >> 3;
#pragma unroll
  for (int i = 0; i < 7; ++i)
    *(LAS v4u *)(c.L + L_A + (tt >> 4) * TILE_D + (kq + 8 * i) * 256 + (tt & 15) * 16) = a[i];
  if (MODE == 1 || MODE == 2) {
    float s1a = 0.f, s1b = 0.f, s2a = 0.f, s2b = 0.f;
#pragma unroll
    for (int i = 0; i < 7; ++i) {
      s1a = dot2(a[i].x, 0x3f803f80u, s1a); s2a = dot2(a[i].x, a[i].x, s2a);
      s1b = dot2(a[i].y, 0x3f803f80u, s1b); s2b = dot2(a[i].y, a[i].y, s2b);
      s1a = dot2(a[i].z, 0x3f803f80u, s1a); s2a = dot2(a[i].z, a[i].z, s2a);
      s1b = dot2(a[i].w, 0x3f803f80u, s1b); s2b = dot2(a[i].w, a[i].w, s2b);
    }
    float s1 = s1a + s1b, s2 = s2a + s2b;
    s1 += dppf<0x128>(s1); s2 += dppf<0x128>(s2);
    s1 = sum_q(s1); s2 = sum_q(s2);
    const LnC lc = ln_coef<MODE == 2>(s1, s2);
    if (kq == 0) *(LAS f32x2 *)(c.L + L_ST + tt * 8) = f32x2{lc.A, lc.B};
  }
  __syncthreads();
}

template <int MODE, int LO, int HI>
DI void gemm_dir(const Ctx &c, int src, int tile, int lw, int jm, f32x4 &r0, f32x4 &r1, LnC &lc, v4u &xk, int pfb, v4u (&pf)[28]) {
  const int lane = c.lane, t16 = lane & 15, q4 = lane >> 4;
  v4u xf[14];
  if (MODE == 3) {
    src = opaque(src);
    int vo[4];
#pragma unroll
    for (int cg = 0; cg < 4; ++cg) {
      const int tok = 16 * tile + t16 + ((cg == 0) ? -1 : (cg == 1) ? 1 : (cg == 2) ? -2 : 2);
      vo[cg] = (tok < 0 || tok >= S_) ? 0x7ff00000 : (tok >> 4) * TILE_D + q4 * 256 + (tok & 15) * 16;
    }
#pragma unroll
    for (int ks = 0; ks < 14; ++ks) {
      const int c0 = (4 * ks) / 14, c1 = (4 * ks + 3) / 14;
      const int v = (c0 == c1) ? vo[c0] : ((4 * ks + q4 >= 14 * c1) ? vo[c1] : vo[c0]);
      xf[ks] = LD16C(c.rs, v, src, ks * 1024);
    }
  } else {
    src = opaque(src + tile * TILE_D);
#pragma unroll
    for (int ks = 0; ks < 14; ++ks) xf[ks] = LD16C(c.rs, lane * 16, src, ks * 1024);
  }
  pfb = opaque(pfb);
  const LAS unsigned char *wb_ = c.L + lw + lane * 16;
  v4u wf[3][2];
#pragma unroll
  for (int ks = 0; ks < 2; ++ks) { wf[ks][0] = *(const LAS v4u *)(wb_ + ks * 1024); wf[ks][1] = *(const LAS v4u *)(wb_ + (14 + ks) * 1024); }
  SB();
  r0 = f32x4{0.f, 0.f, 0.f, 0.f}; r1 = r0;
  float s1a = 0.f, s1b = 0.f, s2a = 0.f, s2b = 0.f;
#pragma unroll
  for (int ks = 0; ks < 14; ++ks) {
    if (ks + 2 < 14) {
      wf[(ks + 2) % 3][0] = *(const LAS v4u *)(wb_ + (ks + 2) * 1024);
      wf[(ks + 2) % 3][1] = *(const LAS v4u *)(wb_ + (16 + ks) * 1024);
    }
    SB();
    r0 = mfma32(wf[ks % 3][0], xf[ks], r0);
    r1 = mfma32(wf[ks % 3][1], xf[ks], r1);
    if (MODE == 2) {
      s1a = dot2(xf[ks].x, 0x3f803f80u, s1a); s2a = dot2(xf[ks].x, xf[ks].x, s2a);
      s1b = dot2(xf[ks].y, 0x3f803f80u, s1b); s2b = dot2(xf[ks].y, xf[ks].y, s2b);
      s1a = dot2(xf[ks].z, 0x3f803f80u, s1a); s2a = dot2(xf[ks].z, xf[ks].z, s2a);
      s1b = dot2(xf[ks].w, 0x3f803f80u, s1b); s2b = dot2(xf[ks].w, xf[ks].w, s2b);
      if (ks == jm) xk = xf[ks];
    }
    SB();
    if (ks >= 7) {
#pragma unroll
      for (int i = LO + ((ks - 7) * (HI - LO)) / 7; i < LO + ((ks - 6) * (HI - LO)) / 7; ++i) pf[i] = LD16C(c.rs, lane * 16, pfb, i * 1024);
    }
  }
  SB();
  if (MODE == 2) lc = ln_coef<true>(sum_q(s1a + s1b), sum_q(s2a + s2b));
}
DI v4u add_res(v4u x, f32x4 a, f32x4 b) {
  v4u o;
  o.x = pk2(bflo(x.x) + rbf(a[0]), bfhi(x.x) + rbf(a[1])); o.y = pk2(bflo(x.y) + rbf(a[2]), bfhi(x.y) + rbf(a[3]));
  o.z = pk2(bflo(x.z) + rbf(b[0]), bfhi(x.z) + rbf(b[1])); o.w = pk2(bflo(x.w) + rbf(b[2]), bfhi(x.w) + rbf(b[3]));
  return o;
}

DI void dual_softmax(f32x4 p0, f32x4 p1, bool v1, v4u &pos, v4u &neg) {
  float pv[8];
#pragma unroll
  for (int i = 0; i < 4; ++i) { pv[i] = rbf(p0[i]) * LOG2E; pv[4 + i] = rbf(p1[i]) * LOG2E; }
  float mx = fmaxf(fmaxf(pv[0], pv[1]), fmaxf(pv[2], pv[3]));
  float mn = fminf(fminf(pv[0], pv[1]), fminf(pv[2], pv[3]));
  const float mx1 = fmaxf(fmaxf(pv[4], pv[5]), fmaxf(pv[6], pv[7]));
  const float mn1 = fminf(fminf(pv[4], pv[5]), fminf(pv[6], pv[7]));
  if (v1) { mx = fmaxf(mx, mx1); mn = fminf(mn, mn1); }
  mx = max_q(mx); mn = min_q(mn);
  float ep[8], en[8];
#pragma unroll
  for (int i = 0; i < 8; ++i) { ep[i] = ex2(pv[i] - mx); en[i] = ex2(mn - pv[i]); }
  float sp = (ep[0] + ep[1]) + (ep[2] + ep[3]), sn = (en[0] + en[1]) + (en[2] + en[3]);
  const float sp1 = (ep[4] + ep[5]) + (ep[6] + ep[7]), sn1 = (en[4] + en[5]) + (en[6] + en[7]);
  if (v1) { sp += sp1; sn += sn1; }
  sp = sum_q(sp); sn = sum_q(sn);
  const float rp = rcp(sp), rn = rcp(sn);
  pos.x = pk2(ep[0] * rp, ep[1] * rp); pos.y = pk2(ep[2] * rp, ep[3] * rp);
  neg.x = pk2(en[0] * rn, en[1] * rn); neg.y = pk2(en[2] * rn, en[3] * rn);
  pos.z = v1 ? pk2(ep[4] * rp, ep[5] * rp) : 0u; pos.w = v1 ? pk2(ep[6] * rp, ep[7] * rp) : 0u;
  neg.z = v1 ? pk2(en[4] * rn, en[5] * rn) : 0u; neg.w = v1 ? pk2(en[6] * rn, en[7] * rn) : 0u;
}
DI v2u trr(const LAS unsigned char *a) {
  return __builtin_bit_cast(v2u, __builtin_amdgcn_ds_read_tr16_b64_v4i16((LAS bf16x4 *)a));
}

__global__ void __launch_bounds__(NTHR, 2) k_main(Params p) {
  extern __shared__ __attribute__((aligned(16))) unsigned char smem[];
  Ctx c;
  c.rs = __builtin_amdgcn_make_buffer_rsrc((void *)p.ws, 0, WS_END, 0x00020000);
  c.ctrl = (gu32 *)p.ws;
  c.L = (LAS unsigned char *)smem;
  c.tid = threadIdx.x; c.lane = c.tid & 63; c.w = __builtin_amdgcn_readfirstlane(c.tid >> 6);
  const int b = blockIdx.x, xcd = b & 7, r = b >> 3;
  const int g = 4 * xcd + (r >> 3), j = r & 7;
  const int tau = 2 * xcd + (r >> 4), head = r & 15;
  const int w = c.w;
  const int jm = r & 15;
  const int jmc = jm < 14 ? jm : 0;
  const f32x4 z4 = {0.f, 0.f, 0.f, 0.f};

  dma_run<84>(p, c, WQKV_T + head * (84 * 1024), L_WQ);

  for (int pair = 0; pair < 2; ++pair) {
    const unsigned e1 = pair + 1;
    const int xin = XIN + pair * ACT_D;
    v4u wr[28];
    v4u phq_p, phq_n;
    {
      FRESH();
      const v4u wph0 = ld16(c.rs, lane * 16, WPHI_T + (head * 2 + 0) * 1024);
      const v4u wph1 = ld16(c.rs, lane * 16, WPHI_T + (head * 2 + 1) * 1024);
      v4u cs[6];
#pragma unroll
      for (int nt = 0; nt < 6; ++nt) cs[nt] = ld16(c.rs, q4 * 16 + nt * 64, CS_QKV + head * 384);
      DRAIN();
      if (pair > 0) wait2(c, C_XN + 2 * tau, C_XN + 2 * tau + 1, 7u * pair);
      else __syncthreads();
      STAMP(0);
      v4u xf[14];
      const int xb = opaque(xin + (tau * 8 + w) * TILE_D);
#pragma unroll
      for (int ks = 0; ks < 14; ++ks) xf[ks] = LD16C(c.rs, lane * 16, xb, ks * 1024);
      const LAS unsigned char *wb_ = c.L + L_WQ + lane * 16;
      float s1a = 0.f, s1b = 0.f, s2a = 0.f, s2b = 0.f;
      f32x4 acc[4];
#pragma unroll
      for (int nt = 0; nt < 4; ++nt) acc[nt] = z4;
      {
        v4u wf[2][4];
#pragma unroll
        for (int nt = 0; nt < 4; ++nt) wf[0][nt] = *(const LAS v4u *)(wb_ + ((2 + nt) * 14) * 1024);
        SB();
#pragma unroll
        for (int ks = 0; ks < 14; ++ks) {
          if (ks + 1 < 14) {
#pragma unroll
            for (int nt = 0; nt < 4; ++nt) wf[(ks + 1) & 1][nt] = *(const LAS v4u *)(wb_ + ((2 + nt) * 14 + ks + 1) * 1024);
          }
          SB();
#pragma unroll
          for (int nt = 0; nt < 4; ++nt) acc[nt] = mfma32(wf[ks & 1][nt], xf[ks], acc[nt]);
          s1a = dot2(xf[ks].x, 0x3f803f80u, s1a); s2a = dot2(xf[ks].x, xf[ks].x, s2a);
          s1b = dot2(xf[ks].y, 0x3f803f80u, s1b); s2b = dot2(xf[ks].y, xf[ks].y, s2b);
          s1a = dot2(xf[ks].z, 0x3f803f80u, s1a); s2a = dot2(xf[ks].z, xf[ks].z, s2a);
          s1b = dot2(xf[ks].w, 0x3f803f80u, s1b); s2b = dot2(xf[ks].w, xf[ks].w, s2b);
          SB();
        }
      }
      STAMP(1);
      const LnC lc = ln_coef<false>(sum_q(s1a + s1b), sum_q(s2a + s2b));
      const bool v1 = q4 < 3;
      {
        v2u kv_[4];
#pragma unroll
        for (int nt = 0; nt < 4; ++nt) {
          const f32x4 v = acc[nt] * lc.A - __builtin_bit_cast(f32x4, cs[2 + nt]) * lc.B;
          kv_[nt] = v2u{pk2(v[0], v[1]), pk2(v[2], v[3])};
        }
        const v4u kp = {kv_[0].x, kv_[0].y, kv_[1].x, kv_[1].y};
        v4u pp, pn;
        dual_softmax(mfma32(wph0, kp, z4), mfma32(wph1, kp, z4), v1, pp, pn);
        LAS unsigned char *pk = c.L + L_PK + (16 * w + t16) * PK_STR + q4 * 8;
        *(LAS v2u *)(pk) = v2u{pp.x, pp.y};
        *(LAS v2u *)(pk + 32) = v2u{pp.z, pp.w};
        *(LAS v2u *)(pk + 64) = v2u{pn.x, pn.y};
        *(LAS v2u *)(pk + 96) = v2u{pn.z, pn.w};
        LAS unsigned char *vv = c.L + L_V + (16 * w + t16) * V_STR + q4 * 8;
        *(LAS v2u *)(vv) = kv_[2];
        *(LAS v2u *)(vv + 32) = v1 ? kv_[3] : v2u{0x00003f80u, 0u};
      }
      __syncthreads();
      STAMP(2);
      {
        const int ft = w >> 1, dt = w & 1;
        const int rq = (lane & 15) >> 2, rp = lane & 3;
        const LAS unsigned char *pa = c.L + L_PK + (8 * q4 + rq) * PK_STR + (16 * ft + 4 * rp) * 2;
        const LAS unsigned char *pb = c.L + L_V + (8 * q4 + rq) * V_STR + (16 * dt + 4 * rp) * 2;
        f32x4 kv = z4;
#pragma unroll
        for (int ks = 0; ks < 4; ++ks) {
          const v2u a0 = trr(pa + (32 * ks) * PK_STR), a1 = trr(pa + (32 * ks + 4) * PK_STR);
          const v2u b0 = trr(pb + (32 * ks) * V_STR), b1 = trr(pb + (32 * ks + 4) * V_STR);
          kv = mfma32(v4u{a0.x, a0.y, a1.x, a1.y}, v4u{b0.x, b0.y, b1.x, b1.y}, kv);
        }
        st16(c.rs, lane * 16, KVP_B + ((pair * 16 + head) * 16 + tau) * 8192 + w * 1024, __builtin_bit_cast(v4u, kv));
      }
      STAMP(3);
      publish(c, C_KV + head);
      STAMP(4);
      dma_run<28>(p, c, WO_T + jmc * (28 * 1024), L_WO);
      {
        f32x4 q0 = z4, q1 = z4;
        v4u wf[3][2];
#pragma unroll
        for (int ks = 0; ks < 2; ++ks) { wf[ks][0] = *(const LAS v4u *)(wb_ + ks * 1024); wf[ks][1] = *(const LAS v4u *)(wb_ + (14 + ks) * 1024); }
        SB();
#pragma unroll
        for (int ks = 0; ks < 14; ++ks) {
          if (ks + 2 < 14) {
            wf[(ks + 2) % 3][0] = *(const LAS v4u *)(wb_ + (ks + 2) * 1024);
            wf[(ks + 2) % 3][1] = *(const LAS v4u *)(wb_ + (16 + ks) * 1024);
          }
          SB();
          q0 = mfma32(wf[ks % 3][0], xf[ks], q0);
          q1 = mfma32(wf[ks % 3][1], xf[ks], q1);
          SB();
        }
        q0 = q0 * lc.A - __builtin_bit_cast(f32x4, cs[0]) * lc.B;
        q1 = q1 * lc.A - __builtin_bit_cast(f32x4, cs[1]) * lc.B;
        const v4u qp = {pk2(q0[0], q0[1]), pk2(q0[2], q0[3]), pk2(q1[0], q1[1]), pk2(q1[2], q1[3])};
        dual_softmax(mfma32(wph0, qp, z4), mfma32(wph1, qp, z4), v1, phq_p, phq_n);
      }
    }
    {
      FRESH();
      wait1(c, C_KV + head, 16u * e1);
      STAMP(5);
      v4u pr[16];
      const int kb = opaque(KVP_B + (pair * 16 + head) * 16 * 8192 + w * 1024);
#pragma unroll
      for (int t = 0; t < 16; ++t) pr[t] = ld16(c.rs, lane * 16, kb + t * 8192);
      SB();
      f32x4 s = __builtin_bit_cast(f32x4, pr[0]);
#pragma unroll
      for (int t = 1; t < 16; ++t) s += __builtin_bit_cast(f32x4, pr[t]);
      {
        const int ft = w >> 1, dt = w & 1, d = 16 * dt + t16, part = ft >> 1, half = ft & 1;
        *(LAS v2u *)(c.L + L_KVS + ((d * 2 + part) * 4 + q4) * 16 + half * 8) = v2u{pk2(s[0], s[1]), pk2(s[2], s[3])};
      }
      __syncthreads();
      f32x4 nm[2];
#pragma unroll
      for (int dt = 0; dt < 2; ++dt) {
        const v4u ap = *(const LAS v4u *)(c.L + L_KVS + (((16 * dt + t16) * 2 + 0) * 4 + q4) * 16);
        const v4u an = *(const LAS v4u *)(c.L + L_KVS + (((16 * dt + t16) * 2 + 1) * 4 + q4) * 16);
        nm[dt] = mfma32(ap, phq_p, z4);
        nm[dt] = mfma32(an, phq_n, nm[dt]);
      }
      const float den0 = __shfl(nm[1][0], 48 + t16);
      const float rden = rcp(rbf(rbf(den0) + 1e-6f));
#pragma unroll
      for (int dt = 0; dt < 2; ++dt) {
        if (dt == 0 || q4 < 3) {
          const int col = DH * head + 16 * dt + 4 * q4;
          v2u o;
          o.x = pk2(rbf(nm[dt][0]) * rden, rbf(nm[dt][1]) * rden);
          o.y = pk2(rbf(nm[dt][2]) * rden, rbf(nm[dt][3]) * rden);
          st8(c.rs, (col >> 3) * 256 + t16 * 16 + ((col >> 2) & 1) * 8, O_B + pair * ACT_D + (tau * 8 + w) * TILE_D, o);
        }
      }
      STAMP(6);
      publish(c, C_O + tau);
      STAMP(7);
      dma_run<28>(p, c, W1_T + jmc * (28 * 1024), L_W1);
      dma_run<28>(p, c, W2_T + jmc * (28 * 1024), L_W2);
    }
    const int w3b = W3_T + (16 * (j < 7 ? j : 0) + 2 * w) * 14 * 1024;
    if (jm < 14) {
      const int tile = tau * 8 + w;
      v4u yk;
      {
        FRESH();
        const int eo = tile * TILE_D + jm * 1024;
        const v4u xr = ld16(c.rs, lane * 16, xin + eo);
        wait1(c, C_O + tau, 16u * e1); STAMP(8);
        f32x4 r0, r1; LnC lc; v4u xk;
        gemm_dir<0, 0, 0>(c, O_B + pair * ACT_D, tile, L_WO, jm, r0, r1, lc, xk, w3b, wr);
        STAMP(9);
        st16(c.rs, lane * 16, XP_B + pair * ACT_D + eo, add_res(xr, r0, r1));
        STAMP(10); publish(c, C_XP + tau); STAMP(11);
        load_run<0, 10, 28>(c, w3b, wr);
      }
      {
        FRESH();
        const int eo = tile * TILE_D + jm * 1024;
        const f32x4 cs0 = __builtin_bit_cast(f32x4, ld16(c.rs, q4 * 32, CS_W1 + jm * 128));
        const f32x4 cs1 = __builtin_bit_cast(f32x4, ld16(c.rs, q4 * 32 + 16, CS_W1 + jm * 128));
        wait1(c, C_XP + tau, 14u * e1); STAMP(12);
        f32x4 r0, r1; LnC lc; v4u xk;
        gemm_dir<2, 0, 0>(c, XP_B + pair * ACT_D, tile, L_W1, jm, r0, r1, lc, xk, w3b, wr);
        STAMP(13);
        r0 = r0 * lc.A - cs0 * lc.B;
        r1 = r1 * lc.A - cs1 * lc.B;
        st16(c.rs, lane * 16, H_B + pair * ACT_D + eo, v4u{gelu2(r0[0], r0[1]), gelu2(r0[2], r0[3]), gelu2(r1[0], r1[1]), gelu2(r1[2], r1[3])});
#pragma unroll
        for (int e = 0; e < 4; ++e) {
          const unsigned d = pk2(bflo(xk[e]) - lc.mb, bfhi(xk[e]) - lc.mb);
          yk[e] = pk2(bflo(d) * lc.r, bfhi(d) * lc.r);
        }
        STAMP(14); publish(c, C_H + tau); STAMP(15);
        load_run<10, 19, 28>(c, w3b, wr);
      }
      {
        FRESH();
        const int eo = tile * TILE_D + jm * 1024;
        wait3(c, C_H + tau, C_H + (tau > 0 ? tau - 1 : tau), C_H + (tau < 15 ? tau + 1 : tau), 14u * e1); STAMP(16);
        f32x4 r0, r1; LnC lc; v4u xk;
        gemm_dir<3, 0, 0>(c, H_B + pair * ACT_D, tile, L_W2, jm, r0, r1, lc, xk, w3b, wr);
        STAMP(17);
        st16(c.rs, lane * 16, X1_B + pair * ACT_D + eo, add_res(yk, r0, r1));
        STAMP(18); publish(c, C_X1 + tau); STAMP(19);
        load_run<19, 28, 28>(c, w3b, wr);
      }
    } else {
      FRESH();
      load_run<0, 28, 28>(c, w3b, wr);
    }
    if (j < 7) {
      {
        FRESH();
        const f32x4 cs0 = __builtin_bit_cast(f32x4, ld16(c.rs, q4 * 32, CS_W3 + (256 * j + 32 * w) * 4));
        const f32x4 cs1 = __builtin_bit_cast(f32x4, ld16(c.rs, q4 * 32 + 16, CS_W3 + (256 * j + 32 * w) * 4));
        wait1(c, C_X1 + (g >> 1), 14u * e1); STAMP(20);
        {
          v4u a[7];
          stage_issue<0>(c, X1_B + pair * ACT_D, g, a);
          stage_finish<1>(c, j, a); STAMP(21);
        }
        f32x4 acc[4][2];
#pragma unroll
        for (int m = 0; m < 4; ++m) { acc[m][0] = z4; acc[m][1] = z4; }
        const int w4b = opaque(W4_T + (4 * j * 56 + 7 * w) * 1024);
        {
          v4u bf[2][4];
          const LAS unsigned char *ab = c.L + L_A + lane * 16;
#pragma unroll
          for (int m = 0; m < 4; ++m) bf[0][m] = *(const LAS v4u *)(ab + m * TILE_D);
#pragma unroll
          for (int ks = 0; ks < 14; ++ks) {
            if (ks + 1 < 14) {
#pragma unroll
              for (int m = 0; m < 4; ++m) bf[(ks + 1) & 1][m] = *(const LAS v4u *)(ab + m * TILE_D + (ks + 1) * 1024);
            }
            SB();
#pragma unroll
            for (int m = 0; m < 4; ++m) {
              acc[m][0] = mfma32(wr[ks], bf[ks & 1][m], acc[m][0]);
              acc[m][1] = mfma32(wr[14 + ks], bf[ks & 1][m], acc[m][1]);
            }
            SB();
            wr[ks] = LD16C(c.rs, lane * 16, w4b, ((ks / 7) * 56 + ks % 7) * 1024);
            wr[14 + ks] = LD16C(c.rs, lane * 16, w4b, ((2 + ks / 7) * 56 + ks % 7) * 1024);
          }
          SB();
        }
#pragma unroll
        for (int m = 0; m < 4; ++m) {
          const f32x2 st = *(const LAS f32x2 *)(c.L + L_ST + (16 * m + t16) * 8);
          const f32x4 u0 = acc[m][0] * st.x - cs0 * st.y, u1 = acc[m][1] * st.x - cs1 * st.y;
          const v4u o = {gelu2(u0[0], u0[1]), gelu2(u0[2], u0[3]), gelu2(u1[0], u1[1]), gelu2(u1[2], u1[3])};
          st16(c.rs, lane * 16, G_B + pair * ACT_F + (4 * g + m) * TILE_F + (32 * j + 4 * w) * 256, o);
        }
        STAMP(22); publish(c, C_G + g); STAMP(23);
      }
      {
        FRESH();
        const int roff = q4 * TILE_D + t16 * 16;
        const int rso = 4 * g * TILE_D + (8 * j + w) * 256;
        const v4u rxp = ld16(c.rs, roff, XP_B + pair * ACT_D + rso);
        const v4u rx1 = ld16(c.rs, roff, X1_B + pair * ACT_D + rso);
        wait1(c, C_G + g, 7u * e1); STAMP(24);
        const int abase = opaque(G_B + pair * ACT_F + 4 * g * TILE_F + 7 * w * 1024);
        constexpr int RING = 14;
        v4u a[RING];
#pragma unroll
        for (int i = 0; i < RING; ++i) a[i] = LD16C(c.rs, lane * 16, abase, (i / 7) * TILE_F + (i % 7) * 1024);
        SB();
#pragma unroll
        for (int m = 0; m < 4; ++m) {
          f32x4 acc[4];
#pragma unroll
          for (int t = 0; t < 4; ++t) acc[t] = z4;
#pragma unroll
          for (int kk = 0; kk < 7; ++kk) {
            const int i = m * 7 + kk;
            const v4u cur = a[i % RING];
            if (i + RING < 28) a[i % RING] = LD16C(c.rs, lane * 16, abase, ((i + RING) / 7) * TILE_F + ((i + RING) % 7) * 1024);
#pragma unroll
            for (int t = 0; t < 4; ++t) acc[t] = mfma32(wr[t * 7 + kk], cur, acc[t]);
            SB();
          }
#pragma unroll
          for (int t = 0; t < 4; ++t) *(LAS f32x4 *)(c.L + L_RED + ((w * 16 + m * 4 + t) * 64 + lane) * 16) = acc[t];
        }
        if (pair == 0) {
          const int qb = opaque(WQKV_T + head * (84 * 1024) + w * 1024);
#pragma unroll
          for (int i = 0; i < 11; ++i) wr[i] = ld16(c.rs, lane * 16, qb + (w + 8 * i < 84 ? i * 8192 : 0));
          SB();
        }
        STAMP(25);
        __syncthreads();
        f32x4 s0 = z4, s1 = z4;
        {
          const int t = w >> 1, qq = 2 * (w & 1);
#pragma unroll
          for (int ww = 0; ww < 8; ++ww) {
            s0 += *(const LAS f32x4 *)(c.L + L_RED + ((ww * 16 + q4 * 4 + t) * 64 + t16 + 16 * qq) * 16);
            s1 += *(const LAS f32x4 *)(c.L + L_RED + ((ww * 16 + q4 * 4 + t) * 64 + t16 + 16 * (qq + 1)) * 16);
          }
        }
        v4u o;
        o.x = pk2(bflo(rxp.x) + rbf(bflo(rx1.x) + rbf(s0[0])), bfhi(rxp.x) + rbf(bfhi(rx1.x) + rbf(s0[1])));
        o.y = pk2(bflo(rxp.y) + rbf(bflo(rx1.y) + rbf(s0[2])), bfhi(rxp.y) + rbf(bfhi(rx1.y) + rbf(s0[3])));
        o.z = pk2(bflo(rxp.z) + rbf(bflo(rx1.z) + rbf(s1[0])), bfhi(rxp.z) + rbf(bfhi(rx1.z) + rbf(s1[1])));
        o.w = pk2(bflo(rxp.w) + rbf(bflo(rx1.w) + rbf(s1[2])), bfhi(rxp.w) + rbf(bfhi(rx1.w) + rbf(s1[3])));
        STAMP(26);
        if (pair == 0) {
          st16(c.rs, roff, XIN + ACT_D + rso, o);
          publish(c, C_XN + g);
#pragma unroll
          for (int i = 0; i < 11; ++i)
            if (w + 8 * i < 84) *(LAS v4u *)(c.L + L_WQ + (w + 8 * i) * 1024 + lane * 16) = wr[i];
        } else {
          *(v4u *)(p.out + (size_t)(64 * g + 16 * q4 + t16) * D_ + 64 * j + 8 * w) = o;
        }
        STAMP(27);
      }
    }
    else if (pair == 0) {
      __syncthreads();
      c.lane = opaque_v(c.lane);
      dma_run<84>(p, c, WQKV_T + head * (84 * 1024), L_WQ);
    }
  }
}

}

extern "C" void kernel_launch(void *const *d_in, const int *in_sizes, int n_in, void *d_out, int out_size, void *d_ws,
                              size_t ws_size, hipStream_t stream) {
  Params p;
  p.x = (const unsigned short *)d_in[0];
  p.wqkv = (const unsigned short *)d_in[1];
  p.wphi = (const unsigned short *)d_in[2];
  p.wo = (const unsigned short *)d_in[3];
  p.w1 = (const unsigned short *)d_in[5];
  p.w2 = (const unsigned short *)d_in[6];
  p.w3 = (const unsigned short *)d_in[7];
  p.w4 = (const unsigned short *)d_in[8];
  p.out = (unsigned short *)d_out;
  p.ws = (unsigned char *)d_ws;
  (void)hipFuncSetAttribute((const void *)k_main, hipFuncAttributeMaxDynamicSharedMemorySize, LDS_BYTES);
  hipLaunchKernelGGL(k_prep, dim3(PREP_BLOCKS), dim3(256), 0, stream, p);
  hipLaunchKernelGGL(k_main, dim3(256), dim3(NTHR), LDS_BYTES, stream, p);
}
```

```cpp
#include <hip/hip_runtime.h>
#include <hip/hip_bf16.h>
#include <stdint.h>

#define GAS __attribute__((address_space(1)))
#define LAS __attribute__((address_space(3)))
#define RLX_AGENT __ATOMIC_RELAXED, __HIP_MEMORY_SCOPE_AGENT
#define DI __device__ __forceinline__
typedef unsigned v4u __attribute__((ext_vector_type(4)));
typedef unsigned v2u __attribute__((ext_vector_type(2)));
typedef float f32x4 __attribute__((ext_vector_type(4)));
typedef float f32x2 __attribute__((ext_vector_type(2)));
typedef short bf16x8 __attribute__((ext_vector_type(8)));
typedef short bf16x4 __attribute__((ext_vector_type(4)));
typedef __bf16 bfv2 __attribute__((ext_vector_type(2)));
typedef GAS unsigned gu32;

namespace {

constexpr int S_ = 2048, D_ = 448, DH = 28, F_ = 1792;
constexpr int NTHR = 512;
constexpr int TILE_D = (D_ / 8) * 256;
constexpr int TILE_F = (F_ / 8) * 256;
constexpr int ACT_D = 128 * TILE_D;
constexpr int ACT_F = 128 * TILE_F;

#define CSTR_W 64
#define POLL_SLEEP 1
constexpr int CSTR = CSTR_W;
constexpr int C_TMO = 0;
constexpr int C_XN = 1;
constexpr int C_KV = C_XN + 32;
constexpr int C_O = C_KV + 16;
constexpr int C_XP = C_O + 16;
constexpr int C_H = C_XP + 16;
constexpr int C_X1 = C_H + 16;
constexpr int C_G = C_X1 + 16;
constexpr int C_NCTR = C_G + 32;
constexpr int CTRL_BYTES = ((C_NCTR * CSTR * 4 + 65535) / 65536) * 65536;
static_assert(C_NCTR * CSTR * 4 <= CTRL_BYTES, "control block");
constexpr int WQKV_T = CTRL_BYTES;
constexpr int WO_T = WQKV_T + 96 * 14 * 1024;
constexpr int W1_T = WO_T + 28 * 14 * 1024;
constexpr int W2_T = W1_T + 28 * 14 * 1024;
constexpr int W3_T = W2_T + 28 * 14 * 1024;
constexpr int W4_T = W3_T + 112 * 14 * 1024;
constexpr int WPHI_T = W4_T + 28 * 56 * 1024;
constexpr int CS_QKV = WPHI_T + 16 * 2 * 1024;
constexpr int CS_W1 = CS_QKV + 96 * 16 * 4;
constexpr int CS_W3 = CS_W1 + 448 * 4;
constexpr int W_END = CS_W3 + 1792 * 4;
constexpr int XIN = (W_END + 4095) & ~4095;
constexpr int O_B = XIN + 2 * ACT_D;
constexpr int XP_B = O_B + 2 * ACT_D;
constexpr int H_B = XP_B + 2 * ACT_D;
constexpr int X1_B = H_B + 2 * ACT_D;
constexpr int G_B = X1_B + 2 * ACT_D;
constexpr int KVP_B = G_B + 2 * ACT_F;
constexpr int WS_END = KVP_B + 2 * 16 * 16 * 8192;

constexpr int L_A = 0;
constexpr int L_ST = 57344;
constexpr int L_RED = 0;
constexpr int L_WQ = 65536;
constexpr int PK_STR = 136, V_STR = 72;
constexpr int L_PK = 0;
constexpr int L_V = 17408;
constexpr int L_KVS = 26624;
constexpr int L_WO = 32768;
constexpr int L_W1 = 65536;
constexpr int L_W2 = 94208;
constexpr int LDS_BYTES = 151552;

#define CODE_PF 22
#define STAGGER 0
#define KVBF16 1
constexpr unsigned kSpinCap = 1u << 22;
#define STAMP(i) do { } while (0)

struct Params {
  const unsigned short *x, *wqkv, *wphi, *wo, *w1, *w2, *w3, *w4;
  unsigned short *out;
  unsigned char *ws;
};

DI float asf(unsigned u) { return __builtin_bit_cast(float, u); }
DI unsigned asu(float f) { return __builtin_bit_cast(unsigned, f); }
DI float bflo(unsigned u) { return asf(u << 16); }
DI float bfhi(unsigned u) { return asf(u & 0xffff0000u); }
DI unsigned pk2(float a, float b) {
  f32x2 v = {a, b};
  return __builtin_bit_cast(unsigned, __builtin_convertvector(v, bfv2));
}
DI float rbf(float a) { return (float)(__bf16)a; }
DI float rcp(float a) { return __builtin_amdgcn_rcpf(a); }
DI float ex2(float a) { return __builtin_amdgcn_exp2f(a); }
DI float dot2(unsigned a, unsigned b, float c) {
  return __builtin_amdgcn_fdot2_f32_bf16(__builtin_bit_cast(bfv2, a), __builtin_bit_cast(bfv2, b), c, false);
}
DI f32x4 mfma32(v4u a, v4u b, f32x4 c) {
  return __builtin_amdgcn_mfma_f32_16x16x32_bf16(__builtin_bit_cast(bf16x8, a), __builtin_bit_cast(bf16x8, b), c, 0, 0, 0);
}
#define DRAIN() asm volatile("s_waitcnt vmcnt(0)" ::: "memory")
constexpr int SC1 = 16;
DI v4u ld16(__amdgpu_buffer_rsrc_t rs, int voff, int soff) { return __builtin_amdgcn_raw_buffer_load_b128(rs, voff, soff, SC1); }
DI v2u ld8(__amdgpu_buffer_rsrc_t rs, int voff, int soff) { return __builtin_amdgcn_raw_buffer_load_b64(rs, voff, soff, SC1); }
DI void st16(__amdgpu_buffer_rsrc_t rs, int voff, int soff, v4u v) { __builtin_amdgcn_raw_buffer_store_b128(v, rs, voff + soff, 0, SC1); }
DI void st8(__amdgpu_buffer_rsrc_t rs, int voff, int soff, v2u v) { __builtin_amdgcn_raw_buffer_store_b64(v, rs, voff, soff, SC1); }
DI void st4(__amdgpu_buffer_rsrc_t rs, int voff, int soff, unsigned v) { __builtin_amdgcn_raw_buffer_store_b32(v, rs, voff, soff, SC1); }
#define LD16C(rs, voff, soff, co) ld16(rs, (voff) + ((co) & 4095), (soff) + ((co) & ~4095))
DI int opaque(int x) { asm volatile("" : "+s"(x)); return x; }
DI int opaque_v(int x) { asm volatile("" : "+v"(x)); return x; }
#define FRESH() c.lane = opaque_v(c.lane); const int lane = c.lane, t16 = lane & 15, q4 = lane >> 4; (void)t16; (void)q4;
#define SB() __builtin_amdgcn_sched_barrier(0)

template <int CTRL> DI float dppf(float v) {
  return asf(__builtin_amdgcn_update_dpp(0, (int)asu(v), CTRL, 0xf, 0xf, false));
}
DI float sum_q(float v) {
  auto r = __builtin_amdgcn_permlane16_swap(asu(v), asu(v), false, false);
  v = asf(r[0]) + asf(r[1]);
  auto s = __builtin_amdgcn_permlane32_swap(asu(v), asu(v), false, false);
  return asf(s[0]) + asf(s[1]);
}
DI float max_q(float v) {
  auto r = __builtin_amdgcn_permlane16_swap(asu(v), asu(v), false, false);
  v = fmaxf(asf(r[0]), asf(r[1]));
  auto s = __builtin_amdgcn_permlane32_swap(asu(v), asu(v), false, false);
  return fmaxf(asf(s[0]), asf(s[1]));
}
DI float min_q(float v) {
  auto r = __builtin_amdgcn_permlane16_swap(asu(v), asu(v), false, false);
  v = fminf(asf(r[0]), asf(r[1]));
  auto s = __builtin_amdgcn_permlane32_swap(asu(v), asu(v), false, false);
  return fminf(asf(s[0]), asf(s[1]));
}

constexpr float LOG2E = 1.4426950408889634f;
DI unsigned gelu2(float a, float b) {
  const f32x2 x = {a, b};
  constexpr float c0 = -2.0f * 0.7978845608028654f * LOG2E, c1 = c0 * 0.044715f;
  const f32x2 u = x * (x * x * c1 + c0);
  f32x2 e = {ex2(u.x), ex2(u.y)};
  e = e + 1.0f;
  const f32x2 r = {rcp(e.x), rcp(e.y)};
  const f32x2 o = x * r;
  return pk2(o.x, o.y);
}

DI void poll_ge(gu32 *w, unsigned want, gu32 *tmo) {
#pragma nounroll
  for (unsigned spins = 0;;) {
    if (__hip_atomic_load(w, RLX_AGENT) >= want) return;
    __builtin_amdgcn_s_sleep(POLL_SLEEP);
    if (++spins > kSpinCap) { __hip_atomic_store(tmo, 1u, RLX_AGENT); return; }
    if ((spins & 1023u) == 0u && __hip_atomic_load(tmo, RLX_AGENT) != 0u) return;
  }
}

struct LnC { float A, B, mb, r; };
template <bool TWICE> DI LnC ln_coef(float s1, float s2) {
  const float mean = s1 * (1.0f / D_);
  const float var = fmaxf(s2 * (1.0f / D_) - mean * mean, 0.f);
  LnC o;
  o.mb = rbf(mean);
  const float sd = rbf(__builtin_amdgcn_sqrtf(rbf(rbf(var) + 1e-5f)));
  o.r = rcp(sd);
  if (TWICE) {
    const float my = (mean - o.mb) * o.r, vy = var * o.r * o.r;
    const float mby = rbf(my);
    const float ry = rcp(rbf(__builtin_amdgcn_sqrtf(rbf(rbf(vy) + 1e-5f))));
    o.A = o.r * ry;
    o.B = (o.mb * o.r + mby) * ry;
  } else {
    o.A = o.r;
    o.B = o.mb * o.r;
  }
  return o;
}

DI float gram_diag(f32x4 gg, int lane) {
  const int t16 = lane & 15, q4 = lane >> 4;
  const float a = (t16 & 1) ? gg[1] : gg[0], b = (t16 & 1) ? gg[3] : gg[2];
  const float d = (t16 & 2) ? b : a;
  return sum_q(q4 == (t16 >> 2) ? d : 0.f);
}

constexpr int NB_X = S_ * (D_ / 8) / 256;
constexpr int NJ_Q = 3 * 4 * 14;
constexpr int NJ_S = 3 * 7 * 14, NJ_3 = 28 * 14, NJ_4 = 7 * 56;
constexpr int NB_W = NJ_Q + NJ_S + NJ_3 + NJ_4;
constexpr int NB_PHI = 8;
constexpr int NB_CS = 21 + 7 + 28;
constexpr int PREP_BLOCKS = NB_W + NB_CS + NB_PHI + NB_X;

__global__ void __launch_bounds__(256) k_prep(Params p) {
  __shared__ __attribute__((aligned(16))) unsigned char sm[32 * 240];
  f32x4 *red = (f32x4 *)sm;
  const __amdgpu_buffer_rsrc_t rs = __builtin_amdgcn_make_buffer_rsrc((void *)p.ws, 0, WS_END, 0x00020000);
  const int tid = threadIdx.x;
  int b = blockIdx.x;
  if (b < NJ_Q) {
    const int ks = b % 14, hg = (b / 14) & 3, part = b / 56;
    const unsigned short *src = p.wqkv + (size_t)(ks * 32) * (3 * D_) + part * D_ + hg * 112;
#pragma unroll
    for (int i = 0; i < 2; ++i) {
      const int ch = tid + 256 * i;
      if (ch < 448) {
        const int row = ch / 14, cc = ch % 14;
        *(v4u *)(sm + row * 240 + cc * 16) = *(const v4u *)(src + (size_t)row * (3 * D_) + cc * 8);
      }
    }
    __syncthreads();
#pragma unroll
    for (int i = 0; i < 2; ++i) {
      const int f = (tid >> 6) + 4 * i, slot = tid & 63, r = slot & 15, kg = slot >> 4;
      const int cl = 16 * (f & 1) + r;
      const bool valid = cl < DH;
      const unsigned short *q_ = (const unsigned short *)(sm + (8 * kg) * 240) + 28 * (f >> 1) + (valid ? cl : 0);
      v4u o;
#pragma unroll
      for (int jj = 0; jj < 4; ++jj) o[jj] = valid ? (unsigned)q_[(2 * jj) * 120] | ((unsigned)q_[(2 * jj + 1) * 120] << 16) : 0u;
      const int nt = (4 * hg + (f >> 1)) * 6 + part * 2 + (f & 1);
      st16(rs, WQKV_T + (nt * 14 + ks) * 1024 + slot * 16, 0, o);
    }
    return;
  }
  if (b < NB_W) {
    int jb = b - NJ_Q;
    const unsigned short *src; int ld, dst, cb, ks, KS; bool perm = true;
    if (jb < NJ_S) {
      const int m = jb / 98, rr = jb % 98;
      cb = rr / 14; ks = rr % 14; KS = 14;
      src = m == 0 ? p.wo : (m == 1 ? p.w1 : p.w2); ld = D_; dst = WO_T + m * (28 * 14 * 1024);
    } else if (jb < NJ_S + NJ_3) {
      jb -= NJ_S; cb = jb / 14; ks = jb % 14; KS = 14; src = p.w3; ld = F_; dst = W3_T;
    } else {
      jb -= NJ_S + NJ_3; cb = jb / 56; ks = jb % 56; KS = 56; src = p.w4; ld = D_; dst = W4_T; perm = false;
    }
    {
      const int row = tid >> 3, cc = tid & 7;
      *(v4u *)(sm + row * 144 + cc * 16) = *(const v4u *)(src + (size_t)(ks * 32 + row) * ld + cb * 64 + cc * 8);
    }
    __syncthreads();
    const int f = tid >> 6, slot = tid & 63, r = slot & 15, kg = slot >> 4;
    const int cl = perm ? 32 * (f >> 1) + 8 * (r >> 2) + 4 * (f & 1) + (r & 3) : 16 * f + r;
    const unsigned short *q_ = (const unsigned short *)(sm + (8 * kg) * 144) + cl;
    v4u o;
#pragma unroll
    for (int jj = 0; jj < 4; ++jj) o[jj] = (unsigned)q_[(2 * jj) * 72] | ((unsigned)q_[(2 * jj + 1) * 72] << 16);
    st16(rs, dst + ((4 * cb + f) * KS + ks) * 1024 + slot * 16, 0, o);
    return;
  }
  b -= NB_W;
  if (b < NB_CS) {
    const unsigned short *src; int ld, col0;
    if (b < 21) { src = p.wqkv; ld = 3 * D_; col0 = b * 64; }
    else if (b < 28) { src = p.w1; ld = D_; col0 = (b - 21) * 64; }
    else { src = p.w3; ld = F_; col0 = (b - 28) * 64; }
    const int n4 = tid & 15, ksl = tid >> 4;
    const unsigned short *s0 = src + (size_t)(ksl * 28) * ld + col0 + 4 * n4;
    f32x4 s = {0.f, 0.f, 0.f, 0.f};
#pragma unroll 7
    for (int k = 0; k < 28; ++k) {
      const v2u e = *(const v2u *)(s0 + (size_t)k * ld);
      s += f32x4{bflo(e.x), bfhi(e.x), bflo(e.y), bfhi(e.y)};
    }
    red[tid] = s;
    __syncthreads();
    if (tid < 64) {
      float a = 0.f;
#pragma unroll
      for (int k = 0; k < 16; ++k) a += ((const float *)red)[(k * 16 + (tid >> 2)) * 4 + (tid & 3)];
      const int col = col0 + tid;
      int off;
      if (b < 21) {
        const int part = col / D_, rem = col % D_, head = rem / DH, cc = rem % DH;
        off = CS_QKV + ((head * 6 + part * 2 + (cc >> 4)) * 16 + (cc & 15)) * 4;
      } else if (b < 28) off = CS_W1 + col * 4;
      else off = CS_W3 + col * 4;
      st4(rs, off, 0, asu(a));
    } else if (b == 0 && tid >= 64) {
      const int i = tid - 64;
      if (i < 192) st4(rs, CS_QKV + (((i >> 2) * 2 + 1) * 16 + 12 + (i & 3)) * 4, 0, 0u);
    }
    return;
  }
  b -= NB_CS;
  if (b < NB_PHI) {
    const int pid = b * 256 + tid, fr = pid >> 6, lane = pid & 63, r = lane & 15, kg = lane >> 4;
    const int h = fr >> 1, f = (fr & 1) * 16 + r;
    unsigned short e[8];
#pragma unroll
    for (int j = 0; j < 8; ++j) {
      const int d = (j < 4) ? 4 * kg + j : 16 + 4 * kg + (j - 4);
      e[j] = (d < DH && f < DH) ? p.wphi[(h * DH + d) * DH + f] : (unsigned short)0;
    }
    v4u v;
    v.x = e[0] | ((unsigned)e[1] << 16); v.y = e[2] | ((unsigned)e[3] << 16);
    v.z = e[4] | ((unsigned)e[5] << 16); v.w = e[6] | ((unsigned)e[7] << 16);
    st16(rs, WPHI_T + pid * 16, 0, v);
    if (pid < C_NCTR) __hip_atomic_store((gu32 *)p.ws + CSTR * pid, 0u, RLX_AGENT);
    return;
  }
  b -= NB_PHI;
  {
    const int fr = b * 4 + (tid >> 6), tile = fr / 14, ks = fr % 14, lane = tid & 63;
    const v4u v = *(const v4u *)(p.x + (size_t)(tile * 16 + (lane & 15)) * D_ + ks * 32 + (lane >> 4) * 8);
    st16(rs, XIN + tile * TILE_D + ks * 1024 + lane * 16, 0, v);
  }
}

struct Ctx {
  __amdgpu_buffer_rsrc_t rs;
  gu32 *ctrl;
  LAS unsigned char *L;
  int tid, lane, w;
};

DI void wait1(const Ctx &c, int ctr, unsigned want) {
  if (c.tid == NTHR - 64) {
    poll_ge(c.ctrl + CSTR * ctr, want, c.ctrl);
    __builtin_amdgcn_fence(__ATOMIC_ACQUIRE, "wavefront");
  }
  __syncthreads();
}
DI void wait2(const Ctx &c, int c0, int c1, unsigned want) {
  if (c.tid == NTHR - 64) {
    poll_ge(c.ctrl + CSTR * c0, want, c.ctrl);
    poll_ge(c.ctrl + CSTR * c1, want, c.ctrl);
    __builtin_amdgcn_fence(__ATOMIC_ACQUIRE, "wavefront");
  }
  __syncthreads();
}
DI void wait3(const Ctx &c, int c0, int c1, int c2, unsigned want) {
  if (c.tid == NTHR - 64) {
    poll_ge(c.ctrl + CSTR * c0, want, c.ctrl);
    poll_ge(c.ctrl + CSTR * c1, want, c.ctrl);
    poll_ge(c.ctrl + CSTR * c2, want, c.ctrl);
    __builtin_amdgcn_fence(__ATOMIC_ACQUIRE, "wavefront");
  }
  __syncthreads();
}
DI void publish(const Ctx &c, int ctr) {
  DRAIN();
  __syncthreads();
  if (c.tid == 0) __hip_atomic_fetch_add(c.ctrl + CSTR * ctr, 1u, RLX_AGENT);
}

DI void dma1k(const unsigned char *gsrc, unsigned lds_dst) {
  unsigned keep;
  asm volatile("s_mov_b32 %0, m0\n\ts_mov_b32 m0, %2\n\ts_nop 0\n\tglobal_load_lds_dwordx4 %1, off sc1\n\ts_mov_b32 m0, %0"
               : "=&s"(keep) : "v"(gsrc), "s"(lds_dst) : "memory");
}
template <int NFR> DI void dma_run(const Params &p, const Ctx &c, int src, int dst) {
  const unsigned char *g = p.ws + src + c.w * 1024 + c.lane * 16;
  const unsigned l = (unsigned)(size_t)c.L + dst + c.w * 1024;
#pragma unroll
  for (int i = 0; i < (NFR + 7) / 8; ++i) {
    if (c.w + 8 * i < NFR) dma1k(g + i * 8192, l + i * 8192);
  }
}

template <int LO, int HI, int N> DI void load_run(const Ctx &c, int base, v4u (&w)[N]) {
  base = opaque(base);
#pragma unroll
  for (int i = LO; i < HI; ++i) w[i] = LD16C(c.rs, c.lane * 16, base, i * 1024);
  SB();
}

template <int MODE> DI void stage_issue(const Ctx &c, int srcbase, int g, v4u (&a)[7]) {
  srcbase = opaque(srcbase);
  const int tt = 8 * c.w + (c.lane & 7), kq = c.lane >> 3;
#pragma unroll
  for (int i = 0; i < 7; ++i) {
    if (MODE == 3) {
      const int k8 = kq + 8 * i, cg = k8 / 14;
      const int tok = 64 * g + tt + ((cg == 0) ? -1 : (cg == 1) ? 1 : (cg == 2) ? -2 : 2);
      int off = (tok >> 4) * TILE_D + kq * 256 + (tok & 15) * 16;
      if (tok < 0 || tok >= S_) off = 0x7ff00000;
      a[i] = LD16C(c.rs, off, srcbase, i * 2048);
    } else {
      a[i] = LD16C(c.rs, (tt >> 4) * TILE_D + kq * 256 + (tt & 15) * 16, srcbase + 4 * g * TILE_D, i * 2048);
    }
  }
  SB();
}
template <int MODE> DI void stage_finish(const Ctx &c, int j, v4u (&a)[7]) {
  const int tt = 8 * c.w + (c.lane & 7), kq = c.lane >> 3;
#pragma unroll
  for (int i = 0; i < 7; ++i)
    *(LAS v4u *)(c.L + L_A + (tt >> 4) * TILE_D + (kq + 8 * i) * 256 + (tt & 15) * 16) = a[i];
  if (MODE == 1 || MODE == 2) {
    float s1a = 0.f, s1b = 0.f, s2a = 0.f, s2b = 0.f;
#pragma unroll
    for (int i = 0; i < 7; ++i) {
      s1a = dot2(a[i].x, 0x3f803f80u, s1a); s2a = dot2(a[i].x, a[i].x, s2a);
      s1b = dot2(a[i].y, 0x3f803f80u, s1b); s2b = dot2(a[i].y, a[i].y, s2b);
      s1a = dot2(a[i].z, 0x3f803f80u, s1a); s2a = dot2(a[i].z, a[i].z, s2a);
      s1b = dot2(a[i].w, 0x3f803f80u, s1b); s2b = dot2(a[i].w, a[i].w, s2b);
    }
    float s1 = s1a + s1b, s2 = s2a + s2b;
    s1 += dppf<0x128>(s1); s2 += dppf<0x128>(s2);
    s1 = sum_q(s1); s2 = sum_q(s2);
    const LnC lc = ln_coef<MODE == 2>(s1, s2);
    if (kq == 0) *(LAS f32x2 *)(c.L + L_ST + tt * 8) = f32x2{lc.A, lc.B};
  }
  __syncthreads();
}

template <int MODE, int LO, int HI>
DI void gemm_dir(const Ctx &c, int src, int tile, int lw, int jm, f32x4 &r0, f32x4 &r1, LnC &lc, v4u &xk, int pfb, v4u (&pf)[28]) {
  const int lane = c.lane, t16 = lane & 15, q4 = lane >> 4;
  v4u xf[14];
  if (MODE == 3) {
    src = opaque(src);
    int vo[4];
#pragma unroll
    for (int cg = 0; cg < 4; ++cg) {
      const int tok = 16 * tile + t16 + ((cg == 0) ? -1 : (cg == 1) ? 1 : (cg == 2) ? -2 : 2);
      vo[cg] = (tok < 0 || tok >= S_) ? 0x7ff00000 : (tok >> 4) * TILE_D + q4 * 256 + (tok & 15) * 16;
    }
#pragma unroll
    for (int ks = 0; ks < 14; ++ks) {
      const int c0 = (4 * ks) / 14, c1 = (4 * ks + 3) / 14;
      const int v = (c0 == c1) ? vo[c0] : ((4 * ks + q4 >= 14 * c1) ? vo[c1] : vo[c0]);
      xf[ks] = LD16C(c.rs, v, src, ks * 1024);
    }
  } else {
    src = opaque(src + tile * TILE_D);
#pragma unroll
    for (int ks = 0; ks < 14; ++ks) xf[ks] = LD16C(c.rs, lane * 16, src, ks * 1024);
  }
  pfb = opaque(pfb);
  const LAS unsigned char *wb_ = c.L + lw + lane * 16;
  v4u wf[3][2];
#pragma unroll
  for (int ks = 0; ks < 2; ++ks) { wf[ks][0] = *(const LAS v4u *)(wb_ + ks * 1024); wf[ks][1] = *(const LAS v4u *)(wb_ + (14 + ks) * 1024); }
  SB();
  r0 = f32x4{0.f, 0.f, 0.f, 0.f}; r1 = r0;
  f32x4 ss = r0, gg = r0;
#pragma unroll
  for (int ks = 0; ks < 14; ++ks) {
    if (ks + 2 < 14) {
      wf[(ks + 2) % 3][0] = *(const LAS v4u *)(wb_ + (ks + 2) * 1024);
      wf[(ks + 2) % 3][1] = *(const LAS v4u *)(wb_ + (16 + ks) * 1024);
    }
    SB();
    r0 = mfma32(wf[ks % 3][0], xf[ks], r0);
    r1 = mfma32(wf[ks % 3][1], xf[ks], r1);
    if (MODE == 2) {
      ss = mfma32(v4u{0x3f803f80u, 0x3f803f80u, 0x3f803f80u, 0x3f803f80u}, xf[ks], ss);
      gg = mfma32(xf[ks], xf[ks], gg);
      if (ks == jm) xk = xf[ks];
    }
    SB();
    if (ks >= 7) {
#pragma unroll
      for (int i = LO + ((ks - 7) * (HI - LO)) / 7; i < LO + ((ks - 6) * (HI - LO)) / 7; ++i) pf[i] = LD16C(c.rs, lane * 16, pfb, i * 1024);
    }
  }
  SB();
  if (MODE == 2) lc = ln_coef<true>(ss[0], gram_diag(gg, lane));
}
DI v4u add_res(v4u x, f32x4 a, f32x4 b) {
  v4u o;
  o.x = pk2(bflo(x.x) + rbf(a[0]), bfhi(x.x) + rbf(a[1])); o.y = pk2(bflo(x.y) + rbf(a[2]), bfhi(x.y) + rbf(a[3]));
  o.z = pk2(bflo(x.z) + rbf(b[0]), bfhi(x.z) + rbf(b[1])); o.w = pk2(bflo(x.w) + rbf(b[2]), bfhi(x.w) + rbf(b[3]));
  return o;
}

DI void dual_softmax(f32x4 p0, f32x4 p1, bool v1, v4u &pos, v4u &neg) {
  float pv[8];
#pragma unroll
  for (int i = 0; i < 4; ++i) { pv[i] = rbf(p0[i]) * LOG2E; pv[4 + i] = rbf(p1[i]) * LOG2E; }
  float mx = fmaxf(fmaxf(pv[0], pv[1]), fmaxf(pv[2], pv[3]));
  float mn = fminf(fminf(pv[0], pv[1]), fminf(pv[2], pv[3]));
  const float mx1 = fmaxf(fmaxf(pv[4], pv[5]), fmaxf(pv[6], pv[7]));
  const float mn1 = fminf(fminf(pv[4], pv[5]), fminf(pv[6], pv[7]));
  if (v1) { mx = fmaxf(mx, mx1); mn = fminf(mn, mn1); }
  mx = max_q(mx); mn = min_q(mn);
  float ep[8], en[8];
#pragma unroll
  for (int i = 0; i < 8; ++i) { ep[i] = ex2(pv[i] - mx); en[i] = ex2(mn - pv[i]); }
  float sp = (ep[0] + ep[1]) + (ep[2] + ep[3]), sn = (en[0] + en[1]) + (en[2] + en[3]);
  const float sp1 = (ep[4] + ep[5]) + (ep[6] + ep[7]), sn1 = (en[4] + en[5]) + (en[6] + en[7]);
  if (v1) { sp += sp1; sn += sn1; }
  sp = sum_q(sp); sn = sum_q(sn);
  const float rp = rcp(sp), rn = rcp(sn);
  pos.x = pk2(ep[0] * rp, ep[1] * rp); pos.y = pk2(ep[2] * rp, ep[3] * rp);
  neg.x = pk2(en[0] * rn, en[1] * rn); neg.y = pk2(en[2] * rn, en[3] * rn);
  pos.z = v1 ? pk2(ep[4] * rp, ep[5] * rp) : 0u; pos.w = v1 ? pk2(ep[6] * rp, ep[7] * rp) : 0u;
  neg.z = v1 ? pk2(en[4] * rn, en[5] * rn) : 0u; neg.w = v1 ? pk2(en[6] * rn, en[7] * rn) : 0u;
}
DI v2u trr(const LAS unsigned char *a) {
  return __builtin_bit_cast(v2u, __builtin_amdgcn_ds_read_tr16_b64_v4i16((LAS bf16x4 *)a));
}

__global__ void __launch_bounds__(NTHR, 2) k_main(Params p) {
  extern __shared__ __attribute__((aligned(16))) unsigned char smem[];
  Ctx c;
  c.rs = __builtin_amdgcn_make_buffer_rsrc((void *)p.ws, 0, WS_END, 0x00020000);
  c.ctrl = (gu32 *)p.ws;
  c.L = (LAS unsigned char *)smem;
  c.tid = threadIdx.x; c.lane = c.tid & 63; c.w = __builtin_amdgcn_readfirstlane(c.tid >> 6);
  const int b = blockIdx.x, xcd = b & 7, r = b >> 3;
  const int g = 4 * xcd + (r >> 3), j = r & 7;
  const int tau = 2 * xcd + (r >> 4), head = r & 15;
  const int w = c.w;
  const int jm = r & 15;
  const int jmc = jm < 14 ? jm : 0;
  const f32x4 z4 = {0.f, 0.f, 0.f, 0.f};

  dma_run<84>(p, c, WQKV_T + head * (84 * 1024), L_WQ);
  v4u cpf;
  {
    unsigned long long pc;
    asm volatile("s_getpc_b64 %0" : "=s"(pc));
    const int idx = (r * 8 + w) % CODE_PF;
    cpf = *(const GAS v4u *)((pc & ~255ull) + idx * 1024 + c.lane * 16);
  }

  for (int pair = 0; pair < 2; ++pair) {
    const unsigned e1 = pair + 1;
    const int xin = XIN + pair * ACT_D;
    v4u wr[28];
    v4u phq_p, phq_n;
    {
      FRESH();
      const v4u wph0 = ld16(c.rs, lane * 16, WPHI_T + (head * 2 + 0) * 1024);
      const v4u wph1 = ld16(c.rs, lane * 16, WPHI_T + (head * 2 + 1) * 1024);
      v4u cs[6];
#pragma unroll
      for (int nt = 0; nt < 6; ++nt) cs[nt] = ld16(c.rs, q4 * 16 + nt * 64, CS_QKV + head * 384);
      if (pair > 0) {
        DRAIN();
        wait2(c, C_XN + 2 * tau, C_XN + 2 * tau + 1, 7u * pair);
      }
      STAMP(0);
      v4u xf[14];
      const int xb = opaque(xin + (tau * 8 + w) * TILE_D);
#pragma unroll
      for (int ks = 0; ks < 14; ++ks) xf[ks] = LD16C(c.rs, lane * 16, xb, ks * 1024);
      if (pair == 0) {
        DRAIN();
        __syncthreads();
        asm volatile("" :: "v"(cpf));
      }
      const LAS unsigned char *wb_ = c.L + L_WQ + lane * 16;
      f32x4 ss = z4, gg = z4;
      f32x4 acc[4];
#pragma unroll
      for (int nt = 0; nt < 4; ++nt) acc[nt] = z4;
      {
        v4u wf[2][4];
#pragma unroll
        for (int nt = 0; nt < 4; ++nt) wf[0][nt] = *(const LAS v4u *)(wb_ + ((2 + nt) * 14) * 1024);
        SB();
#pragma unroll
        for (int ks = 0; ks < 14; ++ks) {
          if (ks + 1 < 14) {
#pragma unroll
            for (int nt = 0; nt < 4; ++nt) wf[(ks + 1) & 1][nt] = *(const LAS v4u *)(wb_ + ((2 + nt) * 14 + ks + 1) * 1024);
          }
          SB();
#pragma unroll
          for (int nt = 0; nt < 4; ++nt) acc[nt] = mfma32(wf[ks & 1][nt], xf[ks], acc[nt]);
          ss = mfma32(v4u{0x3f803f80u, 0x3f803f80u, 0x3f803f80u, 0x3f803f80u}, xf[ks], ss);
          gg = mfma32(xf[ks], xf[ks], gg);
          SB();
        }
      }
      STAMP(1);
      const LnC lc = ln_coef<false>(ss[0], gram_diag(gg, lane));
      const bool v1 = q4 < 3;
      {
        v2u kv_[4];
#pragma unroll
        for (int nt = 0; nt < 4; ++nt) {
          const f32x4 v = acc[nt] * lc.A - __builtin_bit_cast(f32x4, cs[2 + nt]) * lc.B;
          kv_[nt] = v2u{pk2(v[0], v[1]), pk2(v[2], v[3])};
        }
        const v4u kp = {kv_[0].x, kv_[0].y, kv_[1].x, kv_[1].y};
        v4u pp, pn;
        dual_softmax(mfma32(wph0, kp, z4), mfma32(wph1, kp, z4), v1, pp, pn);
        LAS unsigned char *pk = c.L + L_PK + (16 * w + t16) * PK_STR + q4 * 8;
        *(LAS v2u *)(pk) = v2u{pp.x, pp.y};
        *(LAS v2u *)(pk + 32) = v2u{pp.z, pp.w};
        *(LAS v2u *)(pk + 64) = v2u{pn.x, pn.y};
        *(LAS v2u *)(pk + 96) = v2u{pn.z, pn.w};
        LAS unsigned char *vv = c.L + L_V + (16 * w + t16) * V_STR + q4 * 8;
        *(LAS v2u *)(vv) = kv_[2];
        *(LAS v2u *)(vv + 32) = v1 ? kv_[3] : v2u{0x00003f80u, 0u};
      }
      __syncthreads();
      STAMP(2);
      {
        const int ft = w >> 1, dt = w & 1;
        const int rq = (lane & 15) >> 2, rp = lane & 3;
        const LAS unsigned char *pa = c.L + L_PK + (8 * q4 + rq) * PK_STR + (16 * ft + 4 * rp) * 2;
        const LAS unsigned char *pb = c.L + L_V + (8 * q4 + rq) * V_STR + (16 * dt + 4 * rp) * 2;
        f32x4 kv = z4;
#pragma unroll
        for (int ks = 0; ks < 4; ++ks) {
          const v2u a0 = trr(pa + (32 * ks) * PK_STR), a1 = trr(pa + (32 * ks + 4) * PK_STR);
          const v2u b0 = trr(pb + (32 * ks) * V_STR), b1 = trr(pb + (32 * ks + 4) * V_STR);
          kv = mfma32(v4u{a0.x, a0.y, a1.x, a1.y}, v4u{b0.x, b0.y, b1.x, b1.y}, kv);
        }
        st8(c.rs, lane * 16 + (tau & 1) * 8, KVP_B + ((pair * 16 + head) * 8 + (tau >> 1)) * 8192 + w * 1024, v2u{pk2(kv[0], kv[1]), pk2(kv[2], kv[3])});
      }
      STAMP(3);
      publish(c, C_KV + head);
      STAMP(4);
      dma_run<28>(p, c, WO_T + jmc * (28 * 1024), L_WO);
      {
        f32x4 q0 = z4, q1 = z4;
        v4u wf[3][2];
#pragma unroll
        for (int ks = 0; ks < 2; ++ks) { wf[ks][0] = *(const LAS v4u *)(wb_ + ks * 1024); wf[ks][1] = *(const LAS v4u *)(wb_ + (14 + ks) * 1024); }
        SB();
#pragma unroll
        for (int ks = 0; ks < 14; ++ks) {
          if (ks + 2 < 14) {
            wf[(ks + 2) % 3][0] = *(const LAS v4u *)(wb_ + (ks + 2) * 1024);
            wf[(ks + 2) % 3][1] = *(const LAS v4u *)(wb_ + (16 + ks) * 1024);
          }
          SB();
          q0 = mfma32(wf[ks % 3][0], xf[ks], q0);
          q1 = mfma32(wf[ks % 3][1], xf[ks], q1);
          SB();
        }
        q0 = q0 * lc.A - __builtin_bit_cast(f32x4, cs[0]) * lc.B;
        q1 = q1 * lc.A - __builtin_bit_cast(f32x4, cs[1]) * lc.B;
        const v4u qp = {pk2(q0[0], q0[1]), pk2(q0[2], q0[3]), pk2(q1[0], q1[1]), pk2(q1[2], q1[3])};
        dual_softmax(mfma32(wph0, qp, z4), mfma32(wph1, qp, z4), v1, phq_p, phq_n);
      }
    }
    {
      FRESH();
      wait1(c, C_KV + head, 16u * e1);
      STAMP(5);
      v4u pr[8];
      const int kb = opaque(KVP_B + (pair * 16 + head) * 8 * 8192 + w * 1024);
#pragma unroll
      for (int t = 0; t < 8; ++t) pr[t] = ld16(c.rs, lane * 16, kb + t * 8192);
      SB();
      f32x4 s = z4;
#pragma unroll
      for (int t = 0; t < 8; ++t) {
        s += f32x4{bflo(pr[t].x), bfhi(pr[t].x), bflo(pr[t].y), bfhi(pr[t].y)};
        s += f32x4{bflo(pr[t].z), bfhi(pr[t].z), bflo(pr[t].w), bfhi(pr[t].w)};
      }
      {
        const int ft = w >> 1, dt = w & 1, d = 16 * dt + t16, part = ft >> 1, half = ft & 1;
        *(LAS v2u *)(c.L + L_KVS + ((d * 2 + part) * 4 + q4) * 16 + half * 8) = v2u{pk2(s[0], s[1]), pk2(s[2], s[3])};
      }
      __syncthreads();
      f32x4 nm[2];
#pragma unroll
      for (int dt = 0; dt < 2; ++dt) {
        const v4u ap = *(const LAS v4u *)(c.L + L_KVS + (((16 * dt + t16) * 2 + 0) * 4 + q4) * 16);
        const v4u an = *(const LAS v4u *)(c.L + L_KVS + (((16 * dt + t16) * 2 + 1) * 4 + q4) * 16);
        nm[dt] = mfma32(ap, phq_p, z4);
        nm[dt] = mfma32(an, phq_n, nm[dt]);
      }
      const float den0 = __shfl(nm[1][0], 48 + t16);
      const float rden = rcp(rbf(rbf(den0) + 1e-6f));
#pragma unroll
      for (int dt = 0; dt < 2; ++dt) {
        if (dt == 0 || q4 < 3) {
          const int col = DH * head + 16 * dt + 4 * q4;
          v2u o;
          o.x = pk2(rbf(nm[dt][0]) * rden, rbf(nm[dt][1]) * rden);
          o.y = pk2(rbf(nm[dt][2]) * rden, rbf(nm[dt][3]) * rden);
          st8(c.rs, (col >> 3) * 256 + t16 * 16 + ((col >> 2) & 1) * 8, O_B + pair * ACT_D + (tau * 8 + w) * TILE_D, o);
        }
      }
      STAMP(6);
      publish(c, C_O + tau);
      STAMP(7);
      dma_run<28>(p, c, W1_T + jmc * (28 * 1024), L_W1);
      dma_run<28>(p, c, W2_T + jmc * (28 * 1024), L_W2);
    }
    const int w3b = W3_T + (16 * (j < 7 ? j : 0) + 2 * w) * 14 * 1024;
    if (jm < 14) {
      const int tile = tau * 8 + w;
      v4u yk;
      {
        FRESH();
        const int eo = tile * TILE_D + jm * 1024;
        const v4u xr = ld16(c.rs, lane * 16, xin + eo);
        wait1(c, C_O + tau, 16u * e1); STAMP(8);
        f32x4 r0, r1; LnC lc; v4u xk;
        gemm_dir<0, 0, 0>(c, O_B + pair * ACT_D, tile, L_WO, jm, r0, r1, lc, xk, w3b, wr);
        STAMP(9);
        st16(c.rs, lane * 16, XP_B + pair * ACT_D + eo, add_res(xr, r0, r1));
        STAMP(10); publish(c, C_XP + tau); STAMP(11);
        load_run<0, 10, 28>(c, w3b, wr);
      }
      {
        FRESH();
        const int eo = tile * TILE_D + jm * 1024;
        const f32x4 cs0 = __builtin_bit_cast(f32x4, ld16(c.rs, q4 * 32, CS_W1 + jm * 128));
        const f32x4 cs1 = __builtin_bit_cast(f32x4, ld16(c.rs, q4 * 32 + 16, CS_W1 + jm * 128));
        wait1(c, C_XP + tau, 14u * e1); STAMP(12);
        f32x4 r0, r1; LnC lc; v4u xk;
        gemm_dir<2, 0, 0>(c, XP_B + pair * ACT_D, tile, L_W1, jm, r0, r1, lc, xk, w3b, wr);
        STAMP(13);
        r0 = r0 * lc.A - cs0 * lc.B;
        r1 = r1 * lc.A - cs1 * lc.B;
        {
          const int k8 = 4 * jm + q4, cg = k8 / 14;
          const int d = (cg == 0) ? 1 : (cg == 1) ? -1 : (cg == 2) ? 2 : -2;
          const int tok = 16 * tile + t16, sl = tok + d;
          const int ho = (sl < 0 || sl >= S_) ? 0x7ff00000 : (sl >> 4) * TILE_D + k8 * 256 + (sl & 15) * 16;
          st16(c.rs, ho, H_B + pair * ACT_D, v4u{gelu2(r0[0], r0[1]), gelu2(r0[2], r0[3]), gelu2(r1[0], r1[1]), gelu2(r1[2], r1[3])});
          const int src = tok - d;
          if (src < 0 || src >= S_) st16(c.rs, (tok >> 4) * TILE_D + k8 * 256 + (tok & 15) * 16, H_B + pair * ACT_D, v4u{0u, 0u, 0u, 0u});
        }
#pragma unroll
        for (int e = 0; e < 4; ++e) {
          const unsigned d = pk2(bflo(xk[e]) - lc.mb, bfhi(xk[e]) - lc.mb);
          yk[e] = pk2(bflo(d) * lc.r, bfhi(d) * lc.r);
        }
        STAMP(14); publish(c, C_H + tau); STAMP(15);
        load_run<10, 19, 28>(c, w3b, wr);
      }
      {
        FRESH();
        const int eo = tile * TILE_D + jm * 1024;
        wait3(c, C_H + tau, C_H + (tau > 0 ? tau - 1 : tau), C_H + (tau < 15 ? tau + 1 : tau), 14u * e1); STAMP(16);
        f32x4 r0, r1; LnC lc; v4u xk;
        gemm_dir<0, 0, 0>(c, H_B + pair * ACT_D, tile, L_W2, jm, r0, r1, lc, xk, w3b, wr);
        STAMP(17);
        st16(c.rs, lane * 16, X1_B + pair * ACT_D + eo, add_res(yk, r0, r1));
        STAMP(18); publish(c, C_X1 + tau); STAMP(19);
        load_run<19, 28, 28>(c, w3b, wr);
      }
    } else {
      FRESH();
      load_run<0, 28, 28>(c, w3b, wr);
    }
    if (j < 7) {
      {
        FRESH();
        const f32x4 cs0 = __builtin_bit_cast(f32x4, ld16(c.rs, q4 * 32, CS_W3 + (256 * j + 32 * w) * 4));
        const f32x4 cs1 = __builtin_bit_cast(f32x4, ld16(c.rs, q4 * 32 + 16, CS_W3 + (256 * j + 32 * w) * 4));
        wait1(c, C_X1 + (g >> 1), 14u * e1); STAMP(20);
        {
          v4u a[7];
          stage_issue<0>(c, X1_B + pair * ACT_D, g, a);
          stage_finish<1>(c, j, a); STAMP(21);
        }
        f32x4 acc[4][2];
#pragma unroll
        for (int m = 0; m < 4; ++m) { acc[m][0] = z4; acc[m][1] = z4; }
        const int w4b = opaque(W4_T + (4 * j * 56 + 7 * w) * 1024);
        {
          v4u bf[2][4];
          const LAS unsigned char *ab = c.L + L_A + lane * 16;
#pragma unroll
          for (int m = 0; m < 4; ++m) bf[0][m] = *(const LAS v4u *)(ab + m * TILE_D);
#pragma unroll
          for (int ks = 0; ks < 14; ++ks) {
            if (ks + 1 < 14) {
#pragma unroll
              for (int m = 0; m < 4; ++m) bf[(ks + 1) & 1][m] = *(const LAS v4u *)(ab + m * TILE_D + (ks + 1) * 1024);
            }
            SB();
#pragma unroll
            for (int m = 0; m < 4; ++m) {
              acc[m][0] = mfma32(wr[ks], bf[ks & 1][m], acc[m][0]);
              acc[m][1] = mfma32(wr[14 + ks], bf[ks & 1][m], acc[m][1]);
            }
            SB();
            wr[ks] = LD16C(c.rs, lane * 16, w4b, ((ks / 7) * 56 + ks % 7) * 1024);
            wr[14 + ks] = LD16C(c.rs, lane * 16, w4b, ((2 + ks / 7) * 56 + ks % 7) * 1024);
          }
          SB();
        }
#pragma unroll
        for (int m = 0; m < 4; ++m) {
          const f32x2 st = *(const LAS f32x2 *)(c.L + L_ST + (16 * m + t16) * 8);
          const f32x4 u0 = acc[m][0] * st.x - cs0 * st.y, u1 = acc[m][1] * st.x - cs1 * st.y;
          const v4u o = {gelu2(u0[0], u0[1]), gelu2(u0[2], u0[3]), gelu2(u1[0], u1[1]), gelu2(u1[2], u1[3])};
          st16(c.rs, lane * 16, G_B + pair * ACT_F + (4 * g + m) * TILE_F + (32 * j + 4 * w) * 256, o);
        }
        STAMP(22); publish(c, C_G + g); STAMP(23);
      }
      {
        FRESH();
        const int roff = q4 * TILE_D + t16 * 16;
        const int rso = 4 * g * TILE_D + (8 * j + w) * 256;
        const v4u rxp = ld16(c.rs, roff, XP_B + pair * ACT_D + rso);
        const v4u rx1 = ld16(c.rs, roff, X1_B + pair * ACT_D + rso);
        wait1(c, C_G + g, 7u * e1); STAMP(24);
        const int abase = opaque(G_B + pair * ACT_F + 4 * g * TILE_F + 7 * w * 1024);
        constexpr int RING = 14;
        v4u a[RING];
#pragma unroll
        for (int i = 0; i < RING; ++i) a[i] = LD16C(c.rs, lane * 16, abase, (i / 7) * TILE_F + (i % 7) * 1024);
        SB();
#pragma unroll
        for (int m = 0; m < 4; ++m) {
          f32x4 acc[4];
#pragma unroll
          for (int t = 0; t < 4; ++t) acc[t] = z4;
#pragma unroll
          for (int kk = 0; kk < 7; ++kk) {
            const int i = m * 7 + kk;
            const v4u cur = a[i % RING];
            if (i + RING < 28) a[i % RING] = LD16C(c.rs, lane * 16, abase, ((i + RING) / 7) * TILE_F + ((i + RING) % 7) * 1024);
#pragma unroll
            for (int t = 0; t < 4; ++t) acc[t] = mfma32(wr[t * 7 + kk], cur, acc[t]);
            SB();
          }
#pragma unroll
          for (int t = 0; t < 4; ++t) *(LAS f32x4 *)(c.L + L_RED + ((w * 16 + m * 4 + t) * 64 + lane) * 16) = acc[t];
        }
        if (pair == 0) {
          const int qb = opaque(WQKV_T + head * (84 * 1024) + w * 1024);
#pragma unroll
          for (int i = 0; i < 11; ++i) wr[i] = ld16(c.rs, lane * 16, qb + (w + 8 * i < 84 ? i * 8192 : 0));
          SB();
        }
        STAMP(25);
        __syncthreads();
        f32x4 s0 = z4, s1 = z4;
        {
          const int t = w >> 1, qq = 2 * (w & 1);
#pragma unroll
          for (int ww = 0; ww < 8; ++ww) {
            s0 += *(const LAS f32x4 *)(c.L + L_RED + ((ww * 16 + q4 * 4 + t) * 64 + t16 + 16 * qq) * 16);
            s1 += *(const LAS f32x4 *)(c.L + L_RED + ((ww * 16 + q4 * 4 + t) * 64 + t16 + 16 * (qq + 1)) * 16);
          }
        }
        v4u o;
        o.x = pk2(bflo(rxp.x) + rbf(bflo(rx1.x) + rbf(s0[0])), bfhi(rxp.x) + rbf(bfhi(rx1.x) + rbf(s0[1])));
        o.y = pk2(bflo(rxp.y) + rbf(bflo(rx1.y) + rbf(s0[2])), bfhi(rxp.y) + rbf(bfhi(rx1.y) + rbf(s0[3])));
        o.z = pk2(bflo(rxp.z) + rbf(bflo(rx1.z) + rbf(s1[0])), bfhi(rxp.z) + rbf(bfhi(rx1.z) + rbf(s1[1])));
        o.w = pk2(bflo(rxp.w) + rbf(bflo(rx1.w) + rbf(s1[2])), bfhi(rxp.w) + rbf(bfhi(rx1.w) + rbf(s1[3])));
        STAMP(26);
        if (pair == 0) {
          st16(c.rs, roff, XIN + ACT_D + rso, o);
          publish(c, C_XN + g);
#pragma unroll
          for (int i = 0; i < 11; ++i)
            if (w + 8 * i < 84) *(LAS v4u *)(c.L + L_WQ + (w + 8 * i) * 1024 + lane * 16) = wr[i];
        } else {
          *(v4u *)(p.out + (size_t)(64 * g + 16 * q4 + t16) * D_ + 64 * j + 8 * w) = o;
        }
        STAMP(27);
      }
    }
    else if (pair == 0) {
      DRAIN();
      __syncthreads();
      c.lane = opaque_v(c.lane);
      dma_run<84>(p, c, WQKV_T + head * (84 * 1024), L_WQ);
    }
  }
}

}

extern "C" void kernel_launch(void *const *d_in, const int *in_sizes, int n_in, void *d_out, int out_size, void *d_ws,
                              size_t ws_size, hipStream_t stream) {
  Params p;
  p.x = (const unsigned short *)d_in[0];
  p.wqkv = (const unsigned short *)d_in[1];
  p.wphi = (const unsigned short *)d_in[2];
  p.wo = (const unsigned short *)d_in[3];
  p.w1 = (const unsigned short *)d_in[5];
  p.w2 = (const unsigned short *)d_in[6];
  p.w3 = (const unsigned short *)d_in[7];
  p.w4 = (const unsigned short *)d_in[8];
  p.out = (unsigned short *)d_out;
  p.ws = (unsigned char *)d_ws;
  (void)hipFuncSetAttribute((const void *)k_main, hipFuncAttributeMaxDynamicSharedMemorySize, LDS_BYTES);
  hipLaunchKernelGGL(k_prep, dim3(PREP_BLOCKS), dim3(256), 0, stream, p);
  hipLaunchKernelGGL(k_main, dim3(256), dim3(NTHR), LDS_BYTES, stream, p);
}
```

```cpp
#include <hip/hip_runtime.h>
#include <hip/hip_bf16.h>
#include <stdint.h>

#define GAS __attribute__((address_space(1)))
#define LAS __attribute__((address_space(3)))
#define RLX_AGENT __ATOMIC_RELAXED, __HIP_MEMORY_SCOPE_AGENT
#define DI __device__ __forceinline__
typedef unsigned v4u __attribute__((ext_vector_type(4)));
typedef unsigned v2u __attribute__((ext_vector_type(2)));
typedef float f32x4 __attribute__((ext_vector_type(4)));
typedef float f32x2 __attribute__((ext_vector_type(2)));
typedef short bf16x8 __attribute__((ext_vector_type(8)));
typedef short bf16x4 __attribute__((ext_vector_type(4)));
typedef __bf16 bfv2 __attribute__((ext_vector_type(2)));
typedef GAS unsigned gu32;

namespace {

constexpr int S_ = 2048, D_ = 448, DH = 28, F_ = 1792;
constexpr int NTHR = 512;
constexpr int TILE_D = (D_ / 8) * 256;
constexpr int TILE_F = (F_ / 8) * 256;
constexpr int ACT_D = 128 * TILE_D;
constexpr int ACT_F = 128 * TILE_F;

#define CSTR_W 64
#define POLL_SLEEP 1
constexpr int CSTR = CSTR_W;
constexpr int C_TMO = 0;
constexpr int C_XN = 1;
constexpr int C_KV = C_XN + 32;
constexpr int C_O = C_KV + 16;
constexpr int C_XP = C_O + 16;
constexpr int C_H = C_XP + 16;
constexpr int C_X1 = C_H + 16;
constexpr int C_G = C_X1 + 16;
constexpr int C_NCTR = C_G + 32;
constexpr int CTRL_BYTES = ((C_NCTR * CSTR * 4 + 65535) / 65536) * 65536;
static_assert(C_NCTR * CSTR * 4 <= CTRL_BYTES, "control block");
constexpr int WQKV_T = CTRL_BYTES;
constexpr int WO_T = WQKV_T + 96 * 14 * 1024;
constexpr int W1_T = WO_T + 28 * 14 * 1024;
constexpr int W2_T = W1_T + 28 * 14 * 1024;
constexpr int W3_T = W2_T + 28 * 14 * 1024;
constexpr int W4_T = W3_T + 112 * 14 * 1024;
constexpr int WPHI_T = W4_T + 28 * 56 * 1024;
constexpr int CS_QKV = WPHI_T + 16 * 2 * 1024;
constexpr int CS_W1 = CS_QKV + 96 * 16 * 4;
constexpr int CS_W3 = CS_W1 + 448 * 4;
constexpr int W_END = CS_W3 + 1792 * 4;
constexpr int XIN = (W_END + 4095) & ~4095;
constexpr int O_B = XIN + 2 * ACT_D;
constexpr int XP_B = O_B + 2 * ACT_D;
constexpr int H_B = XP_B + 2 * ACT_D;
constexpr int X1_B = H_B + 2 * ACT_D;
constexpr int G_B = X1_B + 2 * ACT_D;
constexpr int KVP_B = G_B + 2 * ACT_F;
constexpr int WS_END = KVP_B + 2 * 16 * 16 * 8192;

constexpr int L_A = 0;
constexpr int L_ST = 57344;
constexpr int L_RED = 0;
constexpr int L_WQ = 65536;
constexpr int PK_STR = 136, V_STR = 72;
constexpr int L_PK = 0;
constexpr int L_V = 17408;
constexpr int L_KVS = 26624;
constexpr int L_WO = 32768;
constexpr int L_W1 = 65536;
constexpr int L_W2 = 94208;
constexpr int LDS_BYTES = 151552;

#define CODE_PF 22
#define TLB_WARM 65536
#define STAGGER 0
#define KVBF16 1
constexpr unsigned kSpinCap = 1u << 22;
#define STAMP(i) do { } while (0)

struct Params {
  const unsigned short *x, *wqkv, *wphi, *wo, *w1, *w2, *w3, *w4;
  unsigned short *out;
  unsigned char *ws;
};

DI float asf(unsigned u) { return __builtin_bit_cast(float, u); }
DI unsigned asu(float f) { return __builtin_bit_cast(unsigned, f); }
DI float bflo(unsigned u) { return asf(u << 16); }
DI float bfhi(unsigned u) { return asf(u & 0xffff0000u); }
DI unsigned pk2(float a, float b) {
  f32x2 v = {a, b};
  return __builtin_bit_cast(unsigned, __builtin_convertvector(v, bfv2));
}
DI float rbf(float a) { return (float)(__bf16)a; }
DI float rcp(float a) { return __builtin_amdgcn_rcpf(a); }
DI float ex2(float a) { return __builtin_amdgcn_exp2f(a); }
DI float dot2(unsigned a, unsigned b, float c) {
  return __builtin_amdgcn_fdot2_f32_bf16(__builtin_bit_cast(bfv2, a), __builtin_bit_cast(bfv2, b), c, false);
}
DI f32x4 mfma32(v4u a, v4u b, f32x4 c) {
  return __builtin_amdgcn_mfma_f32_16x16x32_bf16(__builtin_bit_cast(bf16x8, a), __builtin_bit_cast(bf16x8, b), c, 0, 0, 0);
}
#define DRAIN() asm volatile("s_waitcnt vmcnt(0)" ::: "memory")
constexpr int SC1 = 16;
DI v4u ld16(__amdgpu_buffer_rsrc_t rs, int voff, int soff) { return __builtin_amdgcn_raw_buffer_load_b128(rs, voff, soff, SC1); }
DI v2u ld8(__amdgpu_buffer_rsrc_t rs, int voff, int soff) { return __builtin_amdgcn_raw_buffer_load_b64(rs, voff, soff, SC1); }
DI void st16(__amdgpu_buffer_rsrc_t rs, int voff, int soff, v4u v) { __builtin_amdgcn_raw_buffer_store_b128(v, rs, voff + soff, 0, SC1); }
DI void st8(__amdgpu_buffer_rsrc_t rs, int voff, int soff, v2u v) { __builtin_amdgcn_raw_buffer_store_b64(v, rs, voff, soff, SC1); }
DI void st4(__amdgpu_buffer_rsrc_t rs, int voff, int soff, unsigned v) { __builtin_amdgcn_raw_buffer_store_b32(v, rs, voff, soff, SC1); }
#define LD16C(rs, voff, soff, co) ld16(rs, (voff) + ((co) & 4095), (soff) + ((co) & ~4095))
DI int opaque(int x) { asm volatile("" : "+s"(x)); return x; }
DI int opaque_v(int x) { asm volatile("" : "+v"(x)); return x; }
#define FRESH() c.lane = opaque_v(c.lane); const int lane = c.lane, t16 = lane & 15, q4 = lane >> 4; (void)t16; (void)q4;
#define SB() __builtin_amdgcn_sched_barrier(0)

template <int CTRL> DI float dppf(float v) {
  return asf(__builtin_amdgcn_update_dpp(0, (int)asu(v), CTRL, 0xf, 0xf, false));
}
DI float sum_q(float v) {
  auto r = __builtin_amdgcn_permlane16_swap(asu(v), asu(v), false, false);
  v = asf(r[0]) + asf(r[1]);
  auto s = __builtin_amdgcn_permlane32_swap(asu(v), asu(v), false, false);
  return asf(s[0]) + asf(s[1]);
}
DI float max_q(float v) {
  auto r = __builtin_amdgcn_permlane16_swap(asu(v), asu(v), false, false);
  v = fmaxf(asf(r[0]), asf(r[1]));
  auto s = __builtin_amdgcn_permlane32_swap(asu(v), asu(v), false, false);
  return fmaxf(asf(s[0]), asf(s[1]));
}
DI float min_q(float v) {
  auto r = __builtin_amdgcn_permlane16_swap(asu(v), asu(v), false, false);
  v = fminf(asf(r[0]), asf(r[1]));
  auto s = __builtin_amdgcn_permlane32_swap(asu(v), asu(v), false, false);
  return fminf(asf(s[0]), asf(s[1]));
}

constexpr float LOG2E = 1.4426950408889634f;
DI unsigned gelu2(float a, float b) {
  const f32x2 x = {a, b};
  constexpr float c0 = -2.0f * 0.7978845608028654f * LOG2E, c1 = c0 * 0.044715f;
  const f32x2 u = x * (x * x * c1 + c0);
  f32x2 e = {ex2(u.x), ex2(u.y)};
  e = e + 1.0f;
  const f32x2 r = {rcp(e.x), rcp(e.y)};
  const f32x2 o = x * r;
  return pk2(o.x, o.y);
}

DI void poll_ge(gu32 *w, unsigned want, gu32 *tmo) {
#pragma nounroll
  for (unsigned spins = 0;;) {
    if (__hip_atomic_load(w, RLX_AGENT) >= want) return;
    __builtin_amdgcn_s_sleep(POLL_SLEEP);
    if (++spins > kSpinCap) { __hip_atomic_store(tmo, 1u, RLX_AGENT); return; }
    if ((spins & 1023u) == 0u && __hip_atomic_load(tmo, RLX_AGENT) != 0u) return;
  }
}

struct LnC { float A, B, mb, r; };
template <bool TWICE> DI LnC ln_coef(float s1, float s2) {
  const float mean = s1 * (1.0f / D_);
  const float var = fmaxf(s2 * (1.0f / D_) - mean * mean, 0.f);
  LnC o;
  o.mb = rbf(mean);
  const float sd = rbf(__builtin_amdgcn_sqrtf(rbf(rbf(var) + 1e-5f)));
  o.r = rcp(sd);
  if (TWICE) {
    const float my = (mean - o.mb) * o.r, vy = var * o.r * o.r;
    const float mby = rbf(my);
    const float ry = rcp(rbf(__builtin_amdgcn_sqrtf(rbf(rbf(vy) + 1e-5f))));
    o.A = o.r * ry;
    o.B = (o.mb * o.r + mby) * ry;
  } else {
    o.A = o.r;
    o.B = o.mb * o.r;
  }
  return o;
}

DI float gram_diag(f32x4 gg, int lane) {
  const int t16 = lane & 15, q4 = lane >> 4;
  const float a = (t16 & 1) ? gg[1] : gg[0], b = (t16 & 1) ? gg[3] : gg[2];
  const float d = (t16 & 2) ? b : a;
  return sum_q(q4 == (t16 >> 2) ? d : 0.f);
}

constexpr int NB_X = S_ * (D_ / 8) / 256;
constexpr int NJ_Q = 3 * 4 * 14;
constexpr int NJ_S = 3 * 7 * 14, NJ_3 = 28 * 14, NJ_4 = 7 * 56;
constexpr int NB_W = NJ_Q + NJ_S + NJ_3 + NJ_4;
constexpr int NB_PHI = 8;
constexpr int NB_CS = 21 + 7 + 28;
constexpr int PREP_BLOCKS = NB_W + NB_CS + NB_PHI + NB_X;

__global__ void __launch_bounds__(256) k_prep(Params p) {
  __shared__ __attribute__((aligned(16))) unsigned char sm[32 * 240];
  f32x4 *red = (f32x4 *)sm;
  const __amdgpu_buffer_rsrc_t rs = __builtin_amdgcn_make_buffer_rsrc((void *)p.ws, 0, WS_END, 0x00020000);
  const int tid = threadIdx.x;
  int b = blockIdx.x;
  if (b < 16) {
    unsigned acc = 0u;
#pragma unroll
    for (int i = 0; i < (WS_END / TLB_WARM + 255) / 256; ++i) {
      const int o = (tid + 256 * i) * TLB_WARM;
      acc ^= __builtin_amdgcn_raw_buffer_load_b32(rs, o, 0, 0);
    }
    if (tid * TLB_WARM < S_ * D_ * 2) acc ^= *(const unsigned *)((const unsigned char *)p.out + tid * TLB_WARM);
    asm volatile("" :: "v"(acc));
  }
  if (b < NJ_Q) {
    const int ks = b % 14, hg = (b / 14) & 3, part = b / 56;
    const unsigned short *src = p.wqkv + (size_t)(ks * 32) * (3 * D_) + part * D_ + hg * 112;
#pragma unroll
    for (int i = 0; i < 2; ++i) {
      const int ch = tid + 256 * i;
      if (ch < 448) {
        const int row = ch / 14, cc = ch % 14;
        *(v4u *)(sm + row * 240 + cc * 16) = *(const v4u *)(src + (size_t)row * (3 * D_) + cc * 8);
      }
    }
    __syncthreads();
#pragma unroll
    for (int i = 0; i < 2; ++i) {
      const int f = (tid >> 6) + 4 * i, slot = tid & 63, r = slot & 15, kg = slot >> 4;
      const int cl = 16 * (f & 1) + r;
      const bool valid = cl < DH;
      const unsigned short *q_ = (const unsigned short *)(sm + (8 * kg) * 240) + 28 * (f >> 1) + (valid ? cl : 0);
      v4u o;
#pragma unroll
      for (int jj = 0; jj < 4; ++jj) o[jj] = valid ? (unsigned)q_[(2 * jj) * 120] | ((unsigned)q_[(2 * jj + 1) * 120] << 16) : 0u;
      const int nt = (4 * hg + (f >> 1)) * 6 + part * 2 + (f & 1);
      st16(rs, WQKV_T + (nt * 14 + ks) * 1024 + slot * 16, 0, o);
    }
    return;
  }
  if (b < NB_W) {
    int jb = b - NJ_Q;
    const unsigned short *src; int ld, dst, cb, ks, KS; bool perm = true;
    if (jb < NJ_S) {
      const int m = jb / 98, rr = jb % 98;
      cb = rr / 14; ks = rr % 14; KS = 14;
      src = m == 0 ? p.wo : (m == 1 ? p.w1 : p.w2); ld = D_; dst = WO_T + m * (28 * 14 * 1024);
    } else if (jb < NJ_S + NJ_3) {
      jb -= NJ_S; cb = jb / 14; ks = jb % 14; KS = 14; src = p.w3; ld = F_; dst = W3_T;
    } else {
      jb -= NJ_S + NJ_3; cb = jb / 56; ks = jb % 56; KS = 56; src = p.w4; ld = D_; dst = W4_T; perm = false;
    }
    {
      const int row = tid >> 3, cc = tid & 7;
      *(v4u *)(sm + row * 144 + cc * 16) = *(const v4u *)(src + (size_t)(ks * 32 + row) * ld + cb * 64 + cc * 8);
    }
    __syncthreads();
    const int f = tid >> 6, slot = tid & 63, r = slot & 15, kg = slot >> 4;
    const int cl = perm ? 32 * (f >> 1) + 8 * (r >> 2) + 4 * (f & 1) + (r & 3) : 16 * f + r;
    const unsigned short *q_ = (const unsigned short *)(sm + (8 * kg) * 144) + cl;
    v4u o;
#pragma unroll
    for (int jj = 0; jj < 4; ++jj) o[jj] = (unsigned)q_[(2 * jj) * 72] | ((unsigned)q_[(2 * jj + 1) * 72] << 16);
    st16(rs, dst + ((4 * cb + f) * KS + ks) * 1024 + slot * 16, 0, o);
    return;
  }
  b -= NB_W;
  if (b < NB_CS) {
    const unsigned short *src; int ld, col0;
    if (b < 21) { src = p.wqkv; ld = 3 * D_; col0 = b * 64; }
    else if (b < 28) { src = p.w1; ld = D_; col0 = (b - 21) * 64; }
    else { src = p.w3; ld = F_; col0 = (b - 28) * 64; }
    const int n4 = tid & 15, ksl = tid >> 4;
    const unsigned short *s0 = src + (size_t)(ksl * 28) * ld + col0 + 4 * n4;
    f32x4 s = {0.f, 0.f, 0.f, 0.f};
#pragma unroll 7
    for (int k = 0; k < 28; ++k) {
      const v2u e = *(const v2u *)(s0 + (size_t)k * ld);
      s += f32x4{bflo(e.x), bfhi(e.x), bflo(e.y), bfhi(e.y)};
    }
    red[tid] = s;
    __syncthreads();
    if (tid < 64) {
      float a = 0.f;
#pragma unroll
      for (int k = 0; k < 16; ++k) a += ((const float *)red)[(k * 16 + (tid >> 2)) * 4 + (tid & 3)];
      const int col = col0 + tid;
      int off;
      if (b < 21) {
        const int part = col / D_, rem = col % D_, head = rem / DH, cc = rem % DH;
        off = CS_QKV + ((head * 6 + part * 2 + (cc >> 4)) * 16 + (cc & 15)) * 4;
      } else if (b < 28) off = CS_W1 + col * 4;
      else off = CS_W3 + col * 4;
      st4(rs, off, 0, asu(a));
    } else if (b == 0 && tid >= 64) {
      const int i = tid - 64;
      if (i < 192) st4(rs, CS_QKV + (((i >> 2) * 2 + 1) * 16 + 12 + (i & 3)) * 4, 0, 0u);
    }
    return;
  }
  b -= NB_CS;
  if (b < NB_PHI) {
    const int pid = b * 256 + tid, fr = pid >> 6, lane = pid & 63, r = lane & 15, kg = lane >> 4;
    const int h = fr >> 1, f = (fr & 1) * 16 + r;
    unsigned short e[8];
#pragma unroll
    for (int j = 0; j < 8; ++j) {
      const int d = (j < 4) ? 4 * kg + j : 16 + 4 * kg + (j - 4);
      e[j] = (d < DH && f < DH) ? p.wphi[(h * DH + d) * DH + f] : (unsigned short)0;
    }
    v4u v;
    v.x = e[0] | ((unsigned)e[1] << 16); v.y = e[2] | ((unsigned)e[3] << 16);
    v.z = e[4] | ((unsigned)e[5] << 16); v.w = e[6] | ((unsigned)e[7] << 16);
    st16(rs, WPHI_T + pid * 16, 0, v);
    if (pid < C_NCTR) __hip_atomic_store((gu32 *)p.ws + CSTR * pid, 0u, RLX_AGENT);
    return;
  }
  b -= NB_PHI;
  {
    const int fr = b * 4 + (tid >> 6), tile = fr / 14, ks = fr % 14, lane = tid & 63;
    const v4u v = *(const v4u *)(p.x + (size_t)(tile * 16 + (lane & 15)) * D_ + ks * 32 + (lane >> 4) * 8);
    st16(rs, XIN + tile * TILE_D + ks * 1024 + lane * 16, 0, v);
  }
}

struct Ctx {
  __amdgpu_buffer_rsrc_t rs;
  gu32 *ctrl;
  LAS unsigned char *L;
  int tid, lane, w;
};

DI void wait1(const Ctx &c, int ctr, unsigned want) {
  if (c.tid == NTHR - 64) {
    poll_ge(c.ctrl + CSTR * ctr, want, c.ctrl);
    __builtin_amdgcn_fence(__ATOMIC_ACQUIRE, "wavefront");
  }
  __syncthreads();
}
DI void wait2(const Ctx &c, int c0, int c1, unsigned want) {
  if (c.tid == NTHR - 64) {
    poll_ge(c.ctrl + CSTR * c0, want, c.ctrl);
    poll_ge(c.ctrl + CSTR * c1, want, c.ctrl);
    __builtin_amdgcn_fence(__ATOMIC_ACQUIRE, "wavefront");
  }
  __syncthreads();
}
DI void wait3(const Ctx &c, int c0, int c1, int c2, unsigned want) {
  if (c.tid == NTHR - 64) {
    poll_ge(c.ctrl + CSTR * c0, want, c.ctrl);
    poll_ge(c.ctrl + CSTR * c1, want, c.ctrl);
    poll_ge(c.ctrl + CSTR * c2, want, c.ctrl);
    __builtin_amdgcn_fence(__ATOMIC_ACQUIRE, "wavefront");
  }
  __syncthreads();
}
DI void publish(const Ctx &c, int ctr) {
  DRAIN();
  __syncthreads();
  if (c.tid == 0) __hip_atomic_fetch_add(c.ctrl + CSTR * ctr, 1u, RLX_AGENT);
}

DI void dma1k(const unsigned char *gsrc, unsigned lds_dst) {
  unsigned keep;
  asm volatile("s_mov_b32 %0, m0\n\ts_mov_b32 m0, %2\n\ts_nop 0\n\tglobal_load_lds_dwordx4 %1, off sc1\n\ts_mov_b32 m0, %0"
               : "=&s"(keep) : "v"(gsrc), "s"(lds_dst) : "memory");
}
template <int NFR> DI void dma_run(const Params &p, const Ctx &c, int src, int dst) {
  const unsigned char *g = p.ws + src + c.w * 1024 + c.lane * 16;
  const unsigned l = (unsigned)(size_t)c.L + dst + c.w * 1024;
#pragma unroll
  for (int i = 0; i < (NFR + 7) / 8; ++i) {
    if (c.w + 8 * i < NFR) dma1k(g + i * 8192, l + i * 8192);
  }
}

template <int LO, int HI, int N> DI void load_run(const Ctx &c, int base, v4u (&w)[N]) {
  base = opaque(base);
#pragma unroll
  for (int i = LO; i < HI; ++i) w[i] = LD16C(c.rs, c.lane * 16, base, i * 1024);
  SB();
}

template <int MODE> DI void stage_issue(const Ctx &c, int srcbase, int g, v4u (&a)[7]) {
  srcbase = opaque(srcbase);
  const int tt = 8 * c.w + (c.lane & 7), kq = c.lane >> 3;
#pragma unroll
  for (int i = 0; i < 7; ++i) {
    if (MODE == 3) {
      const int k8 = kq + 8 * i, cg = k8 / 14;
      const int tok = 64 * g + tt + ((cg == 0) ? -1 : (cg == 1) ? 1 : (cg == 2) ? -2 : 2);
      int off = (tok >> 4) * TILE_D + kq * 256 + (tok & 15) * 16;
      if (tok < 0 || tok >= S_) off = 0x7ff00000;
      a[i] = LD16C(c.rs, off, srcbase, i * 2048);
    } else {
      a[i] = LD16C(c.rs, (tt >> 4) * TILE_D + kq * 256 + (tt & 15) * 16, srcbase + 4 * g * TILE_D, i * 2048);
    }
  }
  SB();
}
template <int MODE> DI void stage_finish(const Ctx &c, int j, v4u (&a)[7]) {
  const int tt = 8 * c.w + (c.lane & 7), kq = c.lane >> 3;
#pragma unroll
  for (int i = 0; i < 7; ++i)
    *(LAS v4u *)(c.L + L_A + (tt >> 4) * TILE_D + (kq + 8 * i) * 256 + (tt & 15) * 16) = a[i];
  if (MODE == 1 || MODE == 2) {
    float s1a = 0.f, s1b = 0.f, s2a = 0.f, s2b = 0.f;
#pragma unroll
    for (int i = 0; i < 7; ++i) {
      s1a = dot2(a[i].x, 0x3f803f80u, s1a); s2a = dot2(a[i].x, a[i].x, s2a);
      s1b = dot2(a[i].y, 0x3f803f80u, s1b); s2b = dot2(a[i].y, a[i].y, s2b);
      s1a = dot2(a[i].z, 0x3f803f80u, s1a); s2a = dot2(a[i].z, a[i].z, s2a);
      s1b = dot2(a[i].w, 0x3f803f80u, s1b); s2b = dot2(a[i].w, a[i].w, s2b);
    }
    float s1 = s1a + s1b, s2 = s2a + s2b;
    s1 += dppf<0x128>(s1); s2 += dppf<0x128>(s2);
    s1 = sum_q(s1); s2 = sum_q(s2);
    const LnC lc = ln_coef<MODE == 2>(s1, s2);
    if (kq == 0) *(LAS f32x2 *)(c.L + L_ST + tt * 8) = f32x2{lc.A, lc.B};
  }
  __syncthreads();
}

template <int MODE, int LO, int HI>
DI void gemm_dir(const Ctx &c, int src, int tile, int lw, int jm, f32x4 &r0, f32x4 &r1, LnC &lc, v4u &xk, int pfb, v4u (&pf)[28]) {
  const int lane = c.lane, t16 = lane & 15, q4 = lane >> 4;
  v4u xf[14];
  if (MODE == 3) {
    src = opaque(src);
    int vo[4];
#pragma unroll
    for (int cg = 0; cg < 4; ++cg) {
      const int tok = 16 * tile + t16 + ((cg == 0) ? -1 : (cg == 1) ? 1 : (cg == 2) ? -2 : 2);
      vo[cg] = (tok < 0 || tok >= S_) ? 0x7ff00000 : (tok >> 4) * TILE_D + q4 * 256 + (tok & 15) * 16;
    }
#pragma unroll
    for (int ks = 0; ks < 14; ++ks) {
      const int c0 = (4 * ks) / 14, c1 = (4 * ks + 3) / 14;
      const int v = (c0 == c1) ? vo[c0] : ((4 * ks + q4 >= 14 * c1) ? vo[c1] : vo[c0]);
      xf[ks] = LD16C(c.rs, v, src, ks * 1024);
    }
  } else {
    src = opaque(src + tile * TILE_D);
#pragma unroll
    for (int ks = 0; ks < 14; ++ks) xf[ks] = LD16C(c.rs, lane * 16, src, ks * 1024);
  }
  pfb = opaque(pfb);
  const LAS unsigned char *wb_ = c.L + lw + lane * 16;
  v4u wf[3][2];
#pragma unroll
  for (int ks = 0; ks < 2; ++ks) { wf[ks][0] = *(const LAS v4u *)(wb_ + ks * 1024); wf[ks][1] = *(const LAS v4u *)(wb_ + (14 + ks) * 1024); }
  SB();
  r0 = f32x4{0.f, 0.f, 0.f, 0.f}; r1 = r0;
  f32x4 ss = r0, gg = r0;
#pragma unroll
  for (int ks = 0; ks < 14; ++ks) {
    if (ks + 2 < 14) {
      wf[(ks + 2) % 3][0] = *(const LAS v4u *)(wb_ + (ks + 2) * 1024);
      wf[(ks + 2) % 3][1] = *(const LAS v4u *)(wb_ + (16 + ks) * 1024);
    }
    SB();
    r0 = mfma32(wf[ks % 3][0], xf[ks], r0);
    r1 = mfma32(wf[ks % 3][1], xf[ks], r1);
    if (MODE == 2) {
      ss = mfma32(v4u{0x3f803f80u, 0x3f803f80u, 0x3f803f80u, 0x3f803f80u}, xf[ks], ss);
      gg = mfma32(xf[ks], xf[ks], gg);
      if (ks == jm) xk = xf[ks];
    }
    SB();
    if (ks >= 7) {
#pragma unroll
      for (int i = LO + ((ks - 7) * (HI - LO)) / 7; i < LO + ((ks - 6) * (HI - LO)) / 7; ++i) pf[i] = LD16C(c.rs, lane * 16, pfb, i * 1024);
    }
  }
  SB();
  if (MODE == 2) lc = ln_coef<true>(ss[0], gram_diag(gg, lane));
}
DI v4u add_res(v4u x, f32x4 a, f32x4 b) {
  v4u o;
  o.x = pk2(bflo(x.x) + rbf(a[0]), bfhi(x.x) + rbf(a[1])); o.y = pk2(bflo(x.y) + rbf(a[2]), bfhi(x.y) + rbf(a[3]));
  o.z = pk2(bflo(x.z) + rbf(b[0]), bfhi(x.z) + rbf(b[1])); o.w = pk2(bflo(x.w) + rbf(b[2]), bfhi(x.w) + rbf(b[3]));
  return o;
}

DI void dual_softmax(f32x4 p0, f32x4 p1, bool v1, v4u &pos, v4u &neg) {
  float pv[8];
#pragma unroll
  for (int i = 0; i < 4; ++i) { pv[i] = rbf(p0[i]) * LOG2E; pv[4 + i] = rbf(p1[i]) * LOG2E; }
  float mx = fmaxf(fmaxf(pv[0], pv[1]), fmaxf(pv[2], pv[3]));
  float mn = fminf(fminf(pv[0], pv[1]), fminf(pv[2], pv[3]));
  const float mx1 = fmaxf(fmaxf(pv[4], pv[5]), fmaxf(pv[6], pv[7]));
  const float mn1 = fminf(fminf(pv[4], pv[5]), fminf(pv[6], pv[7]));
  if (v1) { mx = fmaxf(mx, mx1); mn = fminf(mn, mn1); }
  mx = max_q(mx); mn = min_q(mn);
  float ep[8], en[8];
#pragma unroll
  for (int i = 0; i < 8; ++i) { ep[i] = ex2(pv[i] - mx); en[i] = ex2(mn - pv[i]); }
  float sp = (ep[0] + ep[1]) + (ep[2] + ep[3]), sn = (en[0] + en[1]) + (en[2] + en[3]);
  const float sp1 = (ep[4] + ep[5]) + (ep[6] + ep[7]), sn1 = (en[4] + en[5]) + (en[6] + en[7]);
  if (v1) { sp += sp1; sn += sn1; }
  sp = sum_q(sp); sn = sum_q(sn);
  const float rp = rcp(sp), rn = rcp(sn);
  pos.x = pk2(ep[0] * rp, ep[1] * rp); pos.y = pk2(ep[2] * rp, ep[3] * rp);
  neg.x = pk2(en[0] * rn, en[1] * rn); neg.y = pk2(en[2] * rn, en[3] * rn);
  pos.z = v1 ? pk2(ep[4] * rp, ep[5] * rp) : 0u; pos.w = v1 ? pk2(ep[6] * rp, ep[7] * rp) : 0u;
  neg.z = v1 ? pk2(en[4] * rn, en[5] * rn) : 0u; neg.w = v1 ? pk2(en[6] * rn, en[7] * rn) : 0u;
}
DI v2u trr(const LAS unsigned char *a) {
  return __builtin_bit_cast(v2u, __builtin_amdgcn_ds_read_tr16_b64_v4i16((LAS bf16x4 *)a));
}

__global__ void __launch_bounds__(NTHR, 2) k_main(Params p) {
  extern __shared__ __attribute__((aligned(16))) unsigned char smem[];
  Ctx c;
  c.rs = __builtin_amdgcn_make_buffer_rsrc((void *)p.ws, 0, WS_END, 0x00020000);
  c.ctrl = (gu32 *)p.ws;
  c.L = (LAS unsigned char *)smem;
  c.tid = threadIdx.x; c.lane = c.tid & 63; c.w = __builtin_amdgcn_readfirstlane(c.tid >> 6);
  const int b = blockIdx.x, xcd = b & 7, r = b >> 3;
  const int g = 4 * xcd + (r >> 3), j = r & 7;
  const int tau = 2 * xcd + (r >> 4), head = r & 15;
  const int w = c.w;
  const int jm = r & 15;
  const int jmc = jm < 14 ? jm : 0;
  const f32x4 z4 = {0.f, 0.f, 0.f, 0.f};

  dma_run<84>(p, c, WQKV_T + head * (84 * 1024), L_WQ);
  v4u cpf;
  {
    unsigned long long pc;
    asm volatile("s_getpc_b64 %0" : "=s"(pc));
    const int idx = (r * 8 + w) % CODE_PF;
    cpf = *(const GAS v4u *)((pc & ~255ull) + idx * 1024 + c.lane * 16);
  }

  for (int pair = 0; pair < 2; ++pair) {
    const unsigned e1 = pair + 1;
    const int xin = XIN + pair * ACT_D;
    v4u wr[28];
    v4u phq_p, phq_n;
    {
      FRESH();
      const v4u wph0 = ld16(c.rs, lane * 16, WPHI_T + (head * 2 + 0) * 1024);
      const v4u wph1 = ld16(c.rs, lane * 16, WPHI_T + (head * 2 + 1) * 1024);
      v4u cs[6];
#pragma unroll
      for (int nt = 0; nt < 6; ++nt) cs[nt] = ld16(c.rs, q4 * 16 + nt * 64, CS_QKV + head * 384);
      if (pair > 0) {
        DRAIN();
        wait2(c, C_XN + 2 * tau, C_XN + 2 * tau + 1, 7u * pair);
      }
      STAMP(0);
      v4u xf[14];
      const int xb = opaque(xin + (tau * 8 + w) * TILE_D);
#pragma unroll
      for (int ks = 0; ks < 14; ++ks) xf[ks] = LD16C(c.rs, lane * 16, xb, ks * 1024);
      if (pair == 0) {
        DRAIN();
        __syncthreads();
        asm volatile("" :: "v"(cpf));
      }
      const LAS unsigned char *wb_ = c.L + L_WQ + lane * 16;
      f32x4 ss = z4, gg = z4;
      f32x4 acc[4];
#pragma unroll
      for (int nt = 0; nt < 4; ++nt) acc[nt] = z4;
      {
        v4u wf[2][4];
#pragma unroll
        for (int nt = 0; nt < 4; ++nt) wf[0][nt] = *(const LAS v4u *)(wb_ + ((2 + nt) * 14) * 1024);
        SB();
#pragma unroll
        for (int ks = 0; ks < 14; ++ks) {
          if (ks + 1 < 14) {
#pragma unroll
            for (int nt = 0; nt < 4; ++nt) wf[(ks + 1) & 1][nt] = *(const LAS v4u *)(wb_ + ((2 + nt) * 14 + ks + 1) * 1024);
          }
          SB();
#pragma unroll
          for (int nt = 0; nt < 4; ++nt) acc[nt] = mfma32(wf[ks & 1][nt], xf[ks], acc[nt]);
          ss = mfma32(v4u{0x3f803f80u, 0x3f803f80u, 0x3f803f80u, 0x3f803f80u}, xf[ks], ss);
          gg = mfma32(xf[ks], xf[ks], gg);
          SB();
        }
      }
      STAMP(1);
      const LnC lc = ln_coef<false>(ss[0], gram_diag(gg, lane));
      const bool v1 = q4 < 3;
      {
        v2u kv_[4];
#pragma unroll
        for (int nt = 0; nt < 4; ++nt) {
          const f32x4 v = acc[nt] * lc.A - __builtin_bit_cast(f32x4, cs[2 + nt]) * lc.B;
          kv_[nt] = v2u{pk2(v[0], v[1]), pk2(v[2], v[3])};
        }
        const v4u kp = {kv_[0].x, kv_[0].y, kv_[1].x, kv_[1].y};
        v4u pp, pn;
        dual_softmax(mfma32(wph0, kp, z4), mfma32(wph1, kp, z4), v1, pp, pn);
        LAS unsigned char *pk = c.L + L_PK + (16 * w + t16) * PK_STR + q4 * 8;
        *(LAS v2u *)(pk) = v2u{pp.x, pp.y};
        *(LAS v2u *)(pk + 32) = v2u{pp.z, pp.w};
        *(LAS v2u *)(pk + 64) = v2u{pn.x, pn.y};
        *(LAS v2u *)(pk + 96) = v2u{pn.z, pn.w};
        LAS unsigned char *vv = c.L + L_V + (16 * w + t16) * V_STR + q4 * 8;
        *(LAS v2u *)(vv) = kv_[2];
        *(LAS v2u *)(vv + 32) = v1 ? kv_[3] : v2u{0x00003f80u, 0u};
      }
      __syncthreads();
      STAMP(2);
      {
        const int ft = w >> 1, dt = w & 1;
        const int rq = (lane & 15) >> 2, rp = lane & 3;
        const LAS unsigned char *pa = c.L + L_PK + (8 * q4 + rq) * PK_STR + (16 * ft + 4 * rp) * 2;
        const LAS unsigned char *pb = c.L + L_V + (8 * q4 + rq) * V_STR + (16 * dt + 4 * rp) * 2;
        f32x4 kv = z4;
#pragma unroll
        for (int ks = 0; ks < 4; ++ks) {
          const v2u a0 = trr(pa + (32 * ks) * PK_STR), a1 = trr(pa + (32 * ks + 4) * PK_STR);
          const v2u b0 = trr(pb + (32 * ks) * V_STR), b1 = trr(pb + (32 * ks + 4) * V_STR);
          kv = mfma32(v4u{a0.x, a0.y, a1.x, a1.y}, v4u{b0.x, b0.y, b1.x, b1.y}, kv);
        }
        st8(c.rs, lane * 16 + (tau & 1) * 8, KVP_B + ((pair * 16 + head) * 8 + (tau >> 1)) * 8192 + w * 1024, v2u{pk2(kv[0], kv[1]), pk2(kv[2], kv[3])});
      }
      STAMP(3);
      publish(c, C_KV + head);
      STAMP(4);
      dma_run<28>(p, c, WO_T + jmc * (28 * 1024), L_WO);
      {
        f32x4 q0 = z4, q1 = z4;
        v4u wf[3][2];
#pragma unroll
        for (int ks = 0; ks < 2; ++ks) { wf[ks][0] = *(const LAS v4u *)(wb_ + ks * 1024); wf[ks][1] = *(const LAS v4u *)(wb_ + (14 + ks) * 1024); }
        SB();
#pragma unroll
        for (int ks = 0; ks < 14; ++ks) {
          if (ks + 2 < 14) {
            wf[(ks + 2) % 3][0] = *(const LAS v4u *)(wb_ + (ks + 2) * 1024);
            wf[(ks + 2) % 3][1] = *(const LAS v4u *)(wb_ + (16 + ks) * 1024);
          }
          SB();
          q0 = mfma32(wf[ks % 3][0], xf[ks], q0);
          q1 = mfma32(wf[ks % 3][1], xf[ks], q1);
          SB();
        }
        q0 = q0 * lc.A - __builtin_bit_cast(f32x4, cs[0]) * lc.B;
        q1 = q1 * lc.A - __builtin_bit_cast(f32x4, cs[1]) * lc.B;
        const v4u qp = {pk2(q0[0], q0[1]), pk2(q0[2], q0[3]), pk2(q1[0], q1[1]), pk2(q1[2], q1[3])};
        dual_softmax(mfma32(wph0, qp, z4), mfma32(wph1, qp, z4), v1, phq_p, phq_n);
      }
    }
    {
      FRESH();
      wait1(c, C_KV + head, 16u * e1);
      STAMP(5);
      v4u pr[8];
      const int kb = opaque(KVP_B + (pair * 16 + head) * 8 * 8192 + w * 1024);
#pragma unroll
      for (int t = 0; t < 8; ++t) pr[t] = ld16(c.rs, lane * 16, kb + t * 8192);
      SB();
      f32x4 s = z4;
#pragma unroll
      for (int t = 0; t < 8; ++t) {
        s += f32x4{bflo(pr[t].x), bfhi(pr[t].x), bflo(pr[t].y), bfhi(pr[t].y)};
        s += f32x4{bflo(pr[t].z), bfhi(pr[t].z), bflo(pr[t].w), bfhi(pr[t].w)};
      }
      {
        const int ft = w >> 1, dt = w & 1, d = 16 * dt + t16, part = ft >> 1, half = ft & 1;
        *(LAS v2u *)(c.L + L_KVS + ((d * 2 + part) * 4 + q4) * 16 + half * 8) = v2u{pk2(s[0], s[1]), pk2(s[2], s[3])};
      }
      __syncthreads();
      f32x4 nm[2];
#pragma unroll
      for (int dt = 0; dt < 2; ++dt) {
        const v4u ap = *(const LAS v4u *)(c.L + L_KVS + (((16 * dt + t16) * 2 + 0) * 4 + q4) * 16);
        const v4u an = *(const LAS v4u *)(c.L + L_KVS + (((16 * dt + t16) * 2 + 1) * 4 + q4) * 16);
        nm[dt] = mfma32(ap, phq_p, z4);
        nm[dt] = mfma32(an, phq_n, nm[dt]);
      }
      const float den0 = __shfl(nm[1][0], 48 + t16);
      const float rden = rcp(rbf(rbf(den0) + 1e-6f));
#pragma unroll
      for (int dt = 0; dt < 2; ++dt) {
        if (dt == 0 || q4 < 3) {
          const int col = DH * head + 16 * dt + 4 * q4;
          v2u o;
          o.x = pk2(rbf(nm[dt][0]) * rden, rbf(nm[dt][1]) * rden);
          o.y = pk2(rbf(nm[dt][2]) * rden, rbf(nm[dt][3]) * rden);
          st8(c.rs, (col >> 3) * 256 + t16 * 16 + ((col >> 2) & 1) * 8, O_B + pair * ACT_D + (tau * 8 + w) * TILE_D, o);
        }
      }
      STAMP(6);
      publish(c, C_O + tau);
      STAMP(7);
      dma_run<28>(p, c, W1_T + jmc * (28 * 1024), L_W1);
      dma_run<28>(p, c, W2_T + jmc * (28 * 1024), L_W2);
    }
    const int w3b = W3_T + (16 * (j < 7 ? j : 0) + 2 * w) * 14 * 1024;
    if (jm < 14) {
      const int tile = tau * 8 + w;
      v4u yk;
      {
        FRESH();
        const int eo = tile * TILE_D + jm * 1024;
        const v4u xr = ld16(c.rs, lane * 16, xin + eo);
        wait1(c, C_O + tau, 16u * e1); STAMP(8);
        f32x4 r0, r1; LnC lc; v4u xk;
        gemm_dir<0, 0, 0>(c, O_B + pair * ACT_D, tile, L_WO, jm, r0, r1, lc, xk, w3b, wr);
        STAMP(9);
        st16(c.rs, lane * 16, XP_B + pair * ACT_D + eo, add_res(xr, r0, r1));
        STAMP(10); publish(c, C_XP + tau); STAMP(11);
        load_run<0, 10, 28>(c, w3b, wr);
      }
      {
        FRESH();
        const int eo = tile * TILE_D + jm * 1024;
        const f32x4 cs0 = __builtin_bit_cast(f32x4, ld16(c.rs, q4 * 32, CS_W1 + jm * 128));
        const f32x4 cs1 = __builtin_bit_cast(f32x4, ld16(c.rs, q4 * 32 + 16, CS_W1 + jm * 128));
        wait1(c, C_XP + tau, 14u * e1); STAMP(12);
        f32x4 r0, r1; LnC lc; v4u xk;
        gemm_dir<2, 0, 0>(c, XP_B + pair * ACT_D, tile, L_W1, jm, r0, r1, lc, xk, w3b, wr);
        STAMP(13);
        r0 = r0 * lc.A - cs0 * lc.B;
        r1 = r1 * lc.A - cs1 * lc.B;
        {
          const int k8 = 4 * jm + q4, cg = k8 / 14;
          const int d = (cg == 0) ? 1 : (cg == 1) ? -1 : (cg == 2) ? 2 : -2;
          const int tok = 16 * tile + t16, sl = tok + d;
          const int ho = (sl < 0 || sl >= S_) ? 0x7ff00000 : (sl >> 4) * TILE_D + k8 * 256 + (sl & 15) * 16;
          st16(c.rs, ho, H_B + pair * ACT_D, v4u{gelu2(r0[0], r0[1]), gelu2(r0[2], r0[3]), gelu2(r1[0], r1[1]), gelu2(r1[2], r1[3])});
          const int src = tok - d;
          if (src < 0 || src >= S_) st16(c.rs, (tok >> 4) * TILE_D + k8 * 256 + (tok & 15) * 16, H_B + pair * ACT_D, v4u{0u, 0u, 0u, 0u});
        }
#pragma unroll
        for (int e = 0; e < 4; ++e) {
          const unsigned d = pk2(bflo(xk[e]) - lc.mb, bfhi(xk[e]) - lc.mb);
          yk[e] = pk2(bflo(d) * lc.r, bfhi(d) * lc.r);
        }
        STAMP(14); publish(c, C_H + tau); STAMP(15);
        load_run<10, 19, 28>(c, w3b, wr);
      }
      {
        FRESH();
        const int eo = tile * TILE_D + jm * 1024;
        wait3(c, C_H + tau, C_H + (tau > 0 ? tau - 1 : tau), C_H + (tau < 15 ? tau + 1 : tau), 14u * e1); STAMP(16);
        f32x4 r0, r1; LnC lc; v4u xk;
        gemm_dir<0, 0, 0>(c, H_B + pair * ACT_D, tile, L_W2, jm, r0, r1, lc, xk, w3b, wr);
        STAMP(17);
        st16(c.rs, lane * 16, X1_B + pair * ACT_D + eo, add_res(yk, r0, r1));
        STAMP(18); publish(c, C_X1 + tau); STAMP(19);
        load_run<19, 28, 28>(c, w3b, wr);
      }
    } else {
      FRESH();
      load_run<0, 28, 28>(c, w3b, wr);
    }
    if (j < 7) {
      {
        FRESH();
        const f32x4 cs0 = __builtin_bit_cast(f32x4, ld16(c.rs, q4 * 32, CS_W3 + (256 * j + 32 * w) * 4));
        const f32x4 cs1 = __builtin_bit_cast(f32x4, ld16(c.rs, q4 * 32 + 16, CS_W3 + (256 * j + 32 * w) * 4));
        wait1(c, C_X1 + (g >> 1), 14u * e1); STAMP(20);
        {
          v4u a[7];
          stage_issue<0>(c, X1_B + pair * ACT_D, g, a);
          stage_finish<1>(c, j, a); STAMP(21);
        }
        f32x4 acc[4][2];
#pragma unroll
        for (int m = 0; m < 4; ++m) { acc[m][0] = z4; acc[m][1] = z4; }
        const int w4b = opaque(W4_T + (4 * j * 56 + 7 * w) * 1024);
        {
          v4u bf[2][4];
          const LAS unsigned char *ab = c.L + L_A + lane * 16;
#pragma unroll
          for (int m = 0; m < 4; ++m) bf[0][m] = *(const LAS v4u *)(ab + m * TILE_D);
#pragma unroll
          for (int ks = 0; ks < 14; ++ks) {
            if (ks + 1 < 14) {
#pragma unroll
              for (int m = 0; m < 4; ++m) bf[(ks + 1) & 1][m] = *(const LAS v4u *)(ab + m * TILE_D + (ks + 1) * 1024);
            }
            SB();
#pragma unroll
            for (int m = 0; m < 4; ++m) {
              acc[m][0] = mfma32(wr[ks], bf[ks & 1][m], acc[m][0]);
              acc[m][1] = mfma32(wr[14 + ks], bf[ks & 1][m], acc[m][1]);
            }
            SB();
            wr[ks] = LD16C(c.rs, lane * 16, w4b, ((ks / 7) * 56 + ks % 7) * 1024);
            wr[14 + ks] = LD16C(c.rs, lane * 16, w4b, ((2 + ks / 7) * 56 + ks % 7) * 1024);
          }
          SB();
        }
#pragma unroll
        for (int m = 0; m < 4; ++m) {
          const f32x2 st = *(const LAS f32x2 *)(c.L + L_ST + (16 * m + t16) * 8);
          const f32x4 u0 = acc[m][0] * st.x - cs0 * st.y, u1 = acc[m][1] * st.x - cs1 * st.y;
          const v4u o = {gelu2(u0[0], u0[1]), gelu2(u0[2], u0[3]), gelu2(u1[0], u1[1]), gelu2(u1[2], u1[3])};
          st16(c.rs, lane * 16, G_B + pair * ACT_F + (4 * g + m) * TILE_F + (32 * j + 4 * w) * 256, o);
        }
        STAMP(22); publish(c, C_G + g); STAMP(23);
      }
      {
        FRESH();
        const int roff = q4 * TILE_D + t16 * 16;
        const int rso = 4 * g * TILE_D + (8 * j + w) * 256;
        const v4u rxp = ld16(c.rs, roff, XP_B + pair * ACT_D + rso);
        const v4u rx1 = ld16(c.rs, roff, X1_B + pair * ACT_D + rso);
        wait1(c, C_G + g, 7u * e1); STAMP(24);
        const int abase = opaque(G_B + pair * ACT_F + 4 * g * TILE_F + 7 * w * 1024);
        constexpr int RING = 14;
        v4u a[RING];
#pragma unroll
        for (int i = 0; i < RING; ++i) a[i] = LD16C(c.rs, lane * 16, abase, (i / 7) * TILE_F + (i % 7) * 1024);
        SB();
#pragma unroll
        for (int m = 0; m < 4; ++m) {
          f32x4 acc[4];
#pragma unroll
          for (int t = 0; t < 4; ++t) acc[t] = z4;
#pragma unroll
          for (int kk = 0; kk < 7; ++kk) {
            const int i = m * 7 + kk;
            const v4u cur = a[i % RING];
            if (i + RING < 28) a[i % RING] = LD16C(c.rs, lane * 16, abase, ((i + RING) / 7) * TILE_F + ((i + RING) % 7) * 1024);
#pragma unroll
            for (int t = 0; t < 4; ++t) acc[t] = mfma32(wr[t * 7 + kk], cur, acc[t]);
            SB();
          }
#pragma unroll
          for (int t = 0; t < 4; ++t) *(LAS f32x4 *)(c.L + L_RED + ((w * 16 + m * 4 + t) * 64 + lane) * 16) = acc[t];
        }
        if (pair == 0) {
          const int qb = opaque(WQKV_T + head * (84 * 1024) + w * 1024);
#pragma unroll
          for (int i = 0; i < 11; ++i) wr[i] = ld16(c.rs, lane * 16, qb + (w + 8 * i < 84 ? i * 8192 : 0));
          SB();
        }
        STAMP(25);
        __syncthreads();
        f32x4 s0 = z4, s1 = z4;
        {
          const int t = w >> 1, qq = 2 * (w & 1);
#pragma unroll
          for (int ww = 0; ww < 8; ++ww) {
            s0 += *(const LAS f32x4 *)(c.L + L_RED + ((ww * 16 + q4 * 4 + t) * 64 + t16 + 16 * qq) * 16);
            s1 += *(const LAS f32x4 *)(c.L + L_RED + ((ww * 16 + q4 * 4 + t) * 64 + t16 + 16 * (qq + 1)) * 16);
          }
        }
        v4u o;
        o.x = pk2(bflo(rxp.x) + rbf(bflo(rx1.x) + rbf(s0[0])), bfhi(rxp.x) + rbf(bfhi(rx1.x) + rbf(s0[1])));
        o.y = pk2(bflo(rxp.y) + rbf(bflo(rx1.y) + rbf(s0[2])), bfhi(rxp.y) + rbf(bfhi(rx1.y) + rbf(s0[3])));
        o.z = pk2(bflo(rxp.z) + rbf(bflo(rx1.z) + rbf(s1[0])), bfhi(rxp.z) + rbf(bfhi(rx1.z) + rbf(s1[1])));
        o.w = pk2(bflo(rxp.w) + rbf(bflo(rx1.w) + rbf(s1[2])), bfhi(rxp.w) + rbf(bfhi(rx1.w) + rbf(s1[3])));
        STAMP(26);
        if (pair == 0) {
          st16(c.rs, roff, XIN + ACT_D + rso, o);
          publish(c, C_XN + g);
#pragma unroll
          for (int i = 0; i < 11; ++i)
            if (w + 8 * i < 84) *(LAS v4u *)(c.L + L_WQ + (w + 8 * i) * 1024 + lane * 16) = wr[i];
        } else {
          *(v4u *)(p.out + (size_t)(64 * g + 16 * q4 + t16) * D_ + 64 * j + 8 * w) = o;
        }
        STAMP(27);
      }
    }
    else if (pair == 0) {
      DRAIN();
      __syncthreads();
      c.lane = opaque_v(c.lane);
      dma_run<84>(p, c, WQKV_T + head * (84 * 1024), L_WQ);
    }
  }
}

}

extern "C" void kernel_launch(void *const *d_in, const int *in_sizes, int n_in, void *d_out, int out_size, void *d_ws,
                              size_t ws_size, hipStream_t stream) {
  Params p;
  p.x = (const unsigned short *)d_in[0];
  p.wqkv = (const unsigned short *)d_in[1];
  p.wphi = (const unsigned short *)d_in[2];
  p.wo = (const unsigned short *)d_in[3];
  p.w1 = (const unsigned short *)d_in[5];
  p.w2 = (const unsigned short *)d_in[6];
  p.w3 = (const unsigned short *)d_in[7];
  p.w4 = (const unsigned short *)d_in[8];
  p.out = (unsigned short *)d_out;
  p.ws = (unsigned char *)d_ws;
  (void)hipFuncSetAttribute((const void *)k_main, hipFuncAttributeMaxDynamicSharedMemorySize, LDS_BYTES);
  hipLaunchKernelGGL(k_prep, dim3(PREP_BLOCKS), dim3(256), 0, stream, p);
  hipLaunchKernelGGL(k_main, dim3(256), dim3(NTHR), LDS_BYTES, stream, p);
}
```

```cpp
#include <hip/hip_runtime.h>
#include <hip/hip_bf16.h>
#include <stdint.h>

#define GAS __attribute__((address_space(1)))
#define LAS __attribute__((address_space(3)))
#define RLX_AGENT __ATOMIC_RELAXED, __HIP_MEMORY_SCOPE_AGENT
#define DI __device__ __forceinline__
typedef unsigned v4u __attribute__((ext_vector_type(4)));
typedef unsigned v2u __attribute__((ext_vector_type(2)));
typedef float f32x4 __attribute__((ext_vector_type(4)));
typedef float f32x2 __attribute__((ext_vector_type(2)));
typedef short bf16x8 __attribute__((ext_vector_type(8)));
typedef short bf16x4 __attribute__((ext_vector_type(4)));
typedef __bf16 bfv2 __attribute__((ext_vector_type(2)));
typedef GAS unsigned gu32;

namespace {

constexpr int S_ = 2048, D_ = 448, DH = 28, F_ = 1792;
constexpr int NTHR = 512;
constexpr int TILE_D = (D_ / 8) * 256;
constexpr int TILE_F = (F_ / 8) * 256;
constexpr int ACT_D = 128 * TILE_D;
constexpr int ACT_F = 128 * TILE_F;

#define CSTR_W 64
#define POLL_SLEEP 1
constexpr int CSTR = CSTR_W;
constexpr int C_TMO = 0;
constexpr int C_XN = 1;
constexpr int C_KV = C_XN + 32;
constexpr int C_O = C_KV + 16;
constexpr int C_XP = C_O + 16;
constexpr int C_H = C_XP + 16;
constexpr int C_X1 = C_H + 16;
constexpr int C_G = C_X1 + 16;
constexpr int C_NCTR = C_G + 32;
constexpr int CTRL_BYTES = ((C_NCTR * CSTR * 4 + 65535) / 65536) * 65536;
static_assert(C_NCTR * CSTR * 4 <= CTRL_BYTES, "control block");
constexpr int WQKV_T = CTRL_BYTES;
constexpr int WO_T = WQKV_T + 96 * 14 * 1024;
constexpr int W1_T = WO_T + 28 * 14 * 1024;
constexpr int W2_T = W1_T + 28 * 14 * 1024;
constexpr int W3_T = W2_T + 28 * 14 * 1024;
constexpr int W4_T = W3_T + 112 * 14 * 1024;
constexpr int WPHI_T = W4_T + 28 * 56 * 1024;
constexpr int CS_QKV = WPHI_T + 16 * 2 * 1024;
constexpr int CS_W1 = CS_QKV + 96 * 16 * 4;
constexpr int CS_W3 = CS_W1 + 448 * 4;
constexpr int W_END = CS_W3 + 1792 * 4;
constexpr int XIN = (W_END + 4095) & ~4095;
constexpr int O_B = XIN + 2 * ACT_D;
constexpr int XP_B = O_B + 2 * ACT_D;
constexpr int H_B = XP_B + 2 * ACT_D;
constexpr int X1_B = H_B + 2 * ACT_D;
constexpr int G_B = X1_B + 2 * ACT_D;
constexpr int KVP_B = G_B + 2 * ACT_F;
constexpr int WS_END = KVP_B + 2 * 16 * 16 * 8192;

constexpr int L_A = 0;
constexpr int L_ST = 57344;
constexpr int L_RED = 0;
constexpr int L_WQ = 65536;
constexpr int PK_STR = 136, V_STR = 72;
constexpr int L_PK = 0;
constexpr int L_V = 17408;
constexpr int L_KVS = 26624;
constexpr int L_WO = 32768;
constexpr int L_W1 = 65536;
constexpr int L_W2 = 94208;
constexpr int LDS_BYTES = 151552;

#define CODE_PF 22
#define TLB_WARM 0
#define STAGGER 0
#define KVBF16 1
constexpr unsigned kSpinCap = 1u << 22;
#define STAMP(i) do { } while (0)

struct Params {
  const unsigned short *x, *wqkv, *wphi, *wo, *w1, *w2, *w3, *w4;
  unsigned short *out;
  unsigned char *ws;
};

DI float asf(unsigned u) { return __builtin_bit_cast(float, u); }
DI unsigned asu(float f) { return __builtin_bit_cast(unsigned, f); }
DI float bflo(unsigned u) { return asf(u << 16); }
DI float bfhi(unsigned u) { return asf(u & 0xffff0000u); }
DI unsigned pk2(float a, float b) {
  f32x2 v = {a, b};
  return __builtin_bit_cast(unsigned, __builtin_convertvector(v, bfv2));
}
DI float rbf(float a) { return (float)(__bf16)a; }
DI float rcp(float a) { return __builtin_amdgcn_rcpf(a); }
DI float ex2(float a) { return __builtin_amdgcn_exp2f(a); }
DI float dot2(unsigned a, unsigned b, float c) {
  return __builtin_amdgcn_fdot2_f32_bf16(__builtin_bit_cast(bfv2, a), __builtin_bit_cast(bfv2, b), c, false);
}
DI f32x4 mfma32(v4u a, v4u b, f32x4 c) {
  return __builtin_amdgcn_mfma_f32_16x16x32_bf16(__builtin_bit_cast(bf16x8, a), __builtin_bit_cast(bf16x8, b), c, 0, 0, 0);
}
#define DRAIN() asm volatile("s_waitcnt vmcnt(0)" ::: "memory")
constexpr int SC1 = 16;
DI v4u ld16(__amdgpu_buffer_rsrc_t rs, int voff, int soff) { return __builtin_amdgcn_raw_buffer_load_b128(rs, voff, soff, SC1); }
DI v2u ld8(__amdgpu_buffer_rsrc_t rs, int voff, int soff) { return __builtin_amdgcn_raw_buffer_load_b64(rs, voff, soff, SC1); }
DI void st16(__amdgpu_buffer_rsrc_t rs, int voff, int soff, v4u v) { __builtin_amdgcn_raw_buffer_store_b128(v, rs, voff + soff, 0, SC1); }
DI void st8(__amdgpu_buffer_rsrc_t rs, int voff, int soff, v2u v) { __builtin_amdgcn_raw_buffer_store_b64(v, rs, voff, soff, SC1); }
DI void st4(__amdgpu_buffer_rsrc_t rs, int voff, int soff, unsigned v) { __builtin_amdgcn_raw_buffer_store_b32(v, rs, voff, soff, SC1); }
#define LD16C(rs, voff, soff, co) ld16(rs, (voff) + ((co) & 4095), (soff) + ((co) & ~4095))
DI int opaque(int x) { asm volatile("" : "+s"(x)); return x; }
DI int opaque_v(int x) { asm volatile("" : "+v"(x)); return x; }
#define FRESH() c.lane = opaque_v(c.lane); const int lane = c.lane, t16 = lane & 15, q4 = lane >> 4; (void)t16; (void)q4;
#define SB() __builtin_amdgcn_sched_barrier(0)

template <int CTRL> DI float dppf(float v) {
  return asf(__builtin_amdgcn_update_dpp(0, (int)asu(v), CTRL, 0xf, 0xf, false));
}
DI float sum_q(float v) {
  auto r = __builtin_amdgcn_permlane16_swap(asu(v), asu(v), false, false);
  v = asf(r[0]) + asf(r[1]);
  auto s = __builtin_amdgcn_permlane32_swap(asu(v), asu(v), false, false);
  return asf(s[0]) + asf(s[1]);
}
DI float max_q(float v) {
  auto r = __builtin_amdgcn_permlane16_swap(asu(v), asu(v), false, false);
  v = fmaxf(asf(r[0]), asf(r[1]));
  auto s = __builtin_amdgcn_permlane32_swap(asu(v), asu(v), false, false);
  return fmaxf(asf(s[0]), asf(s[1]));
}
DI float min_q(float v) {
  auto r = __builtin_amdgcn_permlane16_swap(asu(v), asu(v), false, false);
  v = fminf(asf(r[0]), asf(r[1]));
  auto s = __builtin_amdgcn_permlane32_swap(asu(v), asu(v), false, false);
  return fminf(asf(s[0]), asf(s[1]));
}

constexpr float LOG2E = 1.4426950408889634f;
DI unsigned gelu2(float a, float b) {
  const f32x2 x = {a, b};
  constexpr float c0 = -2.0f * 0.7978845608028654f * LOG2E, c1 = c0 * 0.044715f;
  const f32x2 u = x * (x * x * c1 + c0);
  f32x2 e = {ex2(u.x), ex2(u.y)};
  e = e + 1.0f;
  const f32x2 r = {rcp(e.x), rcp(e.y)};
  const f32x2 o = x * r;
  return pk2(o.x, o.y);
}

DI void poll_ge(gu32 *w, unsigned want, gu32 *tmo) {
#pragma nounroll
  for (unsigned spins = 0;;) {
    if (__hip_atomic_load(w, RLX_AGENT) >= want) return;
    __builtin_amdgcn_s_sleep(POLL_SLEEP);
    if (++spins > kSpinCap) { __hip_atomic_store(tmo, 1u, RLX_AGENT); return; }
    if ((spins & 1023u) == 0u && __hip_atomic_load(tmo, RLX_AGENT) != 0u) return;
  }
}

struct LnC { float A, B, mb, r; };
template <bool TWICE> DI LnC ln_coef(float s1, float s2) {
  const float mean = s1 * (1.0f / D_);
  const float var = fmaxf(s2 * (1.0f / D_) - mean * mean, 0.f);
  LnC o;
  o.mb = rbf(mean);
  const float sd = rbf(__builtin_amdgcn_sqrtf(rbf(rbf(var) + 1e-5f)));
  o.r = rcp(sd);
  if (TWICE) {
    const float my = (mean - o.mb) * o.r, vy = var * o.r * o.r;
    const float mby = rbf(my);
    const float ry = rcp(rbf(__builtin_amdgcn_sqrtf(rbf(rbf(vy) + 1e-5f))));
    o.A = o.r * ry;
    o.B = (o.mb * o.r + mby) * ry;
  } else {
    o.A = o.r;
    o.B = o.mb * o.r;
  }
  return o;
}

DI float gram_diag(f32x4 gg, int lane) {
  const int t16 = lane & 15, q4 = lane >> 4;
  const float a = (t16 & 1) ? gg[1] : gg[0], b = (t16 & 1) ? gg[3] : gg[2];
  const float d = (t16 & 2) ? b : a;
  return sum_q(q4 == (t16 >> 2) ? d : 0.f);
}

constexpr int NB_X = S_ * (D_ / 8) / 256;
constexpr int NJ_Q = 3 * 4 * 14;
constexpr int NJ_S = 3 * 7 * 14, NJ_3 = 28 * 14, NJ_4 = 7 * 56;
constexpr int NB_W = NJ_Q + NJ_S + NJ_3 + NJ_4;
constexpr int NB_PHI = 8;
constexpr int NB_CS = 21 + 7 + 28;
constexpr int PREP_BLOCKS = NB_W + NB_CS + NB_PHI + NB_X;

__global__ void __launch_bounds__(256) k_prep(Params p) {
  __shared__ __attribute__((aligned(16))) unsigned char sm[32 * 240];
  f32x4 *red = (f32x4 *)sm;
  const __amdgpu_buffer_rsrc_t rs = __builtin_amdgcn_make_buffer_rsrc((void *)p.ws, 0, WS_END, 0x00020000);
  const int tid = threadIdx.x;
  int b = blockIdx.x;
  if (b < NJ_Q) {
    const int ks = b % 14, hg = (b / 14) & 3, part = b / 56;
    const unsigned short *src = p.wqkv + (size_t)(ks * 32) * (3 * D_) + part * D_ + hg * 112;
#pragma unroll
    for (int i = 0; i < 2; ++i) {
      const int ch = tid + 256 * i;
      if (ch < 448) {
        const int row = ch / 14, cc = ch % 14;
        *(v4u *)(sm + row * 240 + cc * 16) = *(const v4u *)(src + (size_t)row * (3 * D_) + cc * 8);
      }
    }
    __syncthreads();
#pragma unroll
    for (int i = 0; i < 2; ++i) {
      const int f = (tid >> 6) + 4 * i, slot = tid & 63, r = slot & 15, kg = slot >> 4;
      const int cl = 16 * (f & 1) + r;
      const bool valid = cl < DH;
      const unsigned short *q_ = (const unsigned short *)(sm + (8 * kg) * 240) + 28 * (f >> 1) + (valid ? cl : 0);
      v4u o;
#pragma unroll
      for (int jj = 0; jj < 4; ++jj) o[jj] = valid ? (unsigned)q_[(2 * jj) * 120] | ((unsigned)q_[(2 * jj + 1) * 120] << 16) : 0u;
      const int nt = (4 * hg + (f >> 1)) * 6 + part * 2 + (f & 1);
      st16(rs, WQKV_T + (nt * 14 + ks) * 1024 + slot * 16, 0, o);
    }
    return;
  }
  if (b < NB_W) {
    int jb = b - NJ_Q;
    const unsigned short *src; int ld, dst, cb, ks, KS; bool perm = true;
    if (jb < NJ_S) {
      const int m = jb / 98, rr = jb % 98;
      cb = rr / 14; ks = rr % 14; KS = 14;
      src = m == 0 ? p.wo : (m == 1 ? p.w1 : p.w2); ld = D_; dst = WO_T + m * (28 * 14 * 1024);
    } else if (jb < NJ_S + NJ_3) {
      jb -= NJ_S; cb = jb / 14; ks = jb % 14; KS = 14; src = p.w3; ld = F_; dst = W3_T;
    } else {
      jb -= NJ_S + NJ_3; cb = jb / 56; ks = jb % 56; KS = 56; src = p.w4; ld = D_; dst = W4_T; perm = false;
    }
    {
      const int row = tid >> 3, cc = tid & 7;
      *(v4u *)(sm + row * 144 + cc * 16) = *(const v4u *)(src + (size_t)(ks * 32 + row) * ld + cb * 64 + cc * 8);
    }
    __syncthreads();
    const int f = tid >> 6, slot = tid & 63, r = slot & 15, kg = slot >> 4;
    const int cl = perm ? 32 * (f >> 1) + 8 * (r >> 2) + 4 * (f & 1) + (r & 3) : 16 * f + r;
    const unsigned short *q_ = (const unsigned short *)(sm + (8 * kg) * 144) + cl;
    v4u o;
#pragma unroll
    for (int jj = 0; jj < 4; ++jj) o[jj] = (unsigned)q_[(2 * jj) * 72] | ((unsigned)q_[(2 * jj + 1) * 72] << 16);
    st16(rs, dst + ((4 * cb + f) * KS + ks) * 1024 + slot * 16, 0, o);
    return;
  }
  b -= NB_W;
  if (b < NB_CS) {
    const unsigned short *src; int ld, col0;
    if (b < 21) { src = p.wqkv; ld = 3 * D_; col0 = b * 64; }
    else if (b < 28) { src = p.w1; ld = D_; col0 = (b - 21) * 64; }
    else { src = p.w3; ld = F_; col0 = (b - 28) * 64; }
    const int n4 = tid & 15, ksl = tid >> 4;
    const unsigned short *s0 = src + (size_t)(ksl * 28) * ld + col0 + 4 * n4;
    f32x4 s = {0.f, 0.f, 0.f, 0.f};
#pragma unroll 7
    for (int k = 0; k < 28; ++k) {
      const v2u e = *(const v2u *)(s0 + (size_t)k * ld);
      s += f32x4{bflo(e.x), bfhi(e.x), bflo(e.y), bfhi(e.y)};
    }
    red[tid] = s;
    __syncthreads();
    if (tid < 64) {
      float a = 0.f;
#pragma unroll
      for (int k = 0; k < 16; ++k) a += ((const float *)red)[(k * 16 + (tid >> 2)) * 4 + (tid & 3)];
      const int col = col0 + tid;
      int off;
      if (b < 21) {
        const int part = col / D_, rem = col % D_, head = rem / DH, cc = rem % DH;
        off = CS_QKV + ((head * 6 + part * 2 + (cc >> 4)) * 16 + (cc & 15)) * 4;
      } else if (b < 28) off = CS_W1 + col * 4;
      else off = CS_W3 + col * 4;
      st4(rs, off, 0, asu(a));
    } else if (b == 0 && tid >= 64) {
      const int i = tid - 64;
      if (i < 192) st4(rs, CS_QKV + (((i >> 2) * 2 + 1) * 16 + 12 + (i & 3)) * 4, 0, 0u);
    }
    return;
  }
  b -= NB_CS;
  if (b < NB_PHI) {
    const int pid = b * 256 + tid, fr = pid >> 6, lane = pid & 63, r = lane & 15, kg = lane >> 4;
    const int h = fr >> 1, f = (fr & 1) * 16 + r;
    unsigned short e[8];
#pragma unroll
    for (int j = 0; j < 8; ++j) {
      const int d = (j < 4) ? 4 * kg + j : 16 + 4 * kg + (j - 4);
      e[j] = (d < DH && f < DH) ? p.wphi[(h * DH + d) * DH + f] : (unsigned short)0;
    }
    v4u v;
    v.x = e[0] | ((unsigned)e[1] << 16); v.y = e[2] | ((unsigned)e[3] << 16);
    v.z = e[4] | ((unsigned)e[5] << 16); v.w = e[6] | ((unsigned)e[7] << 16);
    st16(rs, WPHI_T + pid * 16, 0, v);
    if (pid < C_NCTR) __hip_atomic_store((gu32 *)p.ws + CSTR * pid, 0u, RLX_AGENT);
    return;
  }
  b -= NB_PHI;
  {
    const int fr = b * 4 + (tid >> 6), tile = fr / 14, ks = fr % 14, lane = tid & 63;
    const v4u v = *(const v4u *)(p.x + (size_t)(tile * 16 + (lane & 15)) * D_ + ks * 32 + (lane >> 4) * 8);
    st16(rs, XIN + tile * TILE_D + ks * 1024 + lane * 16, 0, v);
  }
}

struct Ctx {
  __amdgpu_buffer_rsrc_t rs;
  gu32 *ctrl;
  LAS unsigned char *L;
  int tid, lane, w;
};

DI void wait1(const Ctx &c, int ctr, unsigned want) {
  if (c.tid == NTHR - 64) {
    poll_ge(c.ctrl + CSTR * ctr, want, c.ctrl);
    __builtin_amdgcn_fence(__ATOMIC_ACQUIRE, "wavefront");
  }
  __syncthreads();
}
DI void wait2(const Ctx &c, int c0, int c1, unsigned want) {
  if (c.tid == NTHR - 64) {
    poll_ge(c.ctrl + CSTR * c0, want, c.ctrl);
    poll_ge(c.ctrl + CSTR * c1, want, c.ctrl);
    __builtin_amdgcn_fence(__ATOMIC_ACQUIRE, "wavefront");
  }
  __syncthreads();
}
DI void wait3(const Ctx &c, int c0, int c1, int c2, unsigned want) {
  if (c.tid == NTHR - 64) {
    poll_ge(c.ctrl + CSTR * c0, want, c.ctrl);
    poll_ge(c.ctrl + CSTR * c1, want, c.ctrl);
    poll_ge(c.ctrl + CSTR * c2, want, c.ctrl);
    __builtin_amdgcn_fence(__ATOMIC_ACQUIRE, "wavefront");
  }
  __syncthreads();
}
DI void publish(const Ctx &c, int ctr) {
  DRAIN();
  __syncthreads();
  if (c.tid == 0) __hip_atomic_fetch_add(c.ctrl + CSTR * ctr, 1u, RLX_AGENT);
}

DI void dma1k(const unsigned char *gsrc, unsigned lds_dst) {
  unsigned keep;
  asm volatile("s_mov_b32 %0, m0\n\ts_mov_b32 m0, %2\n\ts_nop 0\n\tglobal_load_lds_dwordx4 %1, off sc1\n\ts_mov_b32 m0, %0"
               : "=&s"(keep) : "v"(gsrc), "s"(lds_dst) : "memory");
}
template <int NFR> DI void dma_run(const Params &p, const Ctx &c, int src, int dst) {
  const unsigned char *g = p.ws + src + c.w * 1024 + c.lane * 16;
  const unsigned l = (unsigned)(size_t)c.L + dst + c.w * 1024;
#pragma unroll
  for (int i = 0; i < (NFR + 7) / 8; ++i) {
    if (c.w + 8 * i < NFR) dma1k(g + i * 8192, l + i * 8192);
  }
}

template <int LO, int HI, int N> DI void load_run(const Ctx &c, int base, v4u (&w)[N]) {
  base = opaque(base);
#pragma unroll
  for (int i = LO; i < HI; ++i) w[i] = LD16C(c.rs, c.lane * 16, base, i * 1024);
  SB();
}

template <int MODE> DI void stage_issue(const Ctx &c, int srcbase, int g, v4u (&a)[7]) {
  srcbase = opaque(srcbase);
  const int tt = 8 * c.w + (c.lane & 7), kq = c.lane >> 3;
#pragma unroll
  for (int i = 0; i < 7; ++i) {
    if (MODE == 3) {
      const int k8 = kq + 8 * i, cg = k8 / 14;
      const int tok = 64 * g + tt + ((cg == 0) ? -1 : (cg == 1) ? 1 : (cg == 2) ? -2 : 2);
      int off = (tok >> 4) * TILE_D + kq * 256 + (tok & 15) * 16;
      if (tok < 0 || tok >= S_) off = 0x7ff00000;
      a[i] = LD16C(c.rs, off, srcbase, i * 2048);
    } else {
      a[i] = LD16C(c.rs, (tt >> 4) * TILE_D + kq * 256 + (tt & 15) * 16, srcbase + 4 * g * TILE_D, i * 2048);
    }
  }
  SB();
}
template <int MODE> DI void stage_finish(const Ctx &c, int j, v4u (&a)[7]) {
  const int tt = 8 * c.w + (c.lane & 7), kq = c.lane >> 3;
#pragma unroll
  for (int i = 0; i < 7; ++i)
    *(LAS v4u *)(c.L + L_A + (tt >> 4) * TILE_D + (kq + 8 * i) * 256 + (tt & 15) * 16) = a[i];
  if (MODE == 1 || MODE == 2) {
    float s1a = 0.f, s1b = 0.f, s2a = 0.f, s2b = 0.f;
#pragma unroll
    for (int i = 0; i < 7; ++i) {
      s1a = dot2(a[i].x, 0x3f803f80u, s1a); s2a = dot2(a[i].x, a[i].x, s2a);
      s1b = dot2(a[i].y, 0x3f803f80u, s1b); s2b = dot2(a[i].y, a[i].y, s2b);
      s1a = dot2(a[i].z, 0x3f803f80u, s1a); s2a = dot2(a[i].z, a[i].z, s2a);
      s1b = dot2(a[i].w, 0x3f803f80u, s1b); s2b = dot2(a[i].w, a[i].w, s2b);
    }
    float s1 = s1a + s1b, s2 = s2a + s2b;
    s1 += dppf<0x128>(s1); s2 += dppf<0x128>(s2);
    s1 = sum_q(s1); s2 = sum_q(s2);
    const LnC lc = ln_coef<MODE == 2>(s1, s2);
    if (kq == 0) *(LAS f32x2 *)(c.L + L_ST + tt * 8) = f32x2{lc.A, lc.B};
  }
  __syncthreads();
}

template <int MODE, int LO, int HI>
DI void gemm_dir(const Ctx &c, int src, int tile, int lw, int jm, f32x4 &r0, f32x4 &r1, LnC &lc, v4u &xk, int pfb, v4u (&pf)[28]) {
  const int lane = c.lane, t16 = lane & 15, q4 = lane >> 4;
  v4u xf[14];
  if (MODE == 3) {
    src = opaque(src);
    int vo[4];
#pragma unroll
    for (int cg = 0; cg < 4; ++cg) {
      const int tok = 16 * tile + t16 + ((cg == 0) ? -1 : (cg == 1) ? 1 : (cg == 2) ? -2 : 2);
      vo[cg] = (tok < 0 || tok >= S_) ? 0x7ff00000 : (tok >> 4) * TILE_D + q4 * 256 + (tok & 15) * 16;
    }
#pragma unroll
    for (int ks = 0; ks < 14; ++ks) {
      const int c0 = (4 * ks) / 14, c1 = (4 * ks + 3) / 14;
      const int v = (c0 == c1) ? vo[c0] : ((4 * ks + q4 >= 14 * c1) ? vo[c1] : vo[c0]);
      xf[ks] = LD16C(c.rs, v, src, ks * 1024);
    }
  } else {
    src = opaque(src + tile * TILE_D);
#pragma unroll
    for (int ks = 0; ks < 14; ++ks) xf[ks] = LD16C(c.rs, lane * 16, src, ks * 1024);
  }
  pfb = opaque(pfb);
  const LAS unsigned char *wb_ = c.L + lw + lane * 16;
  v4u wf[3][2];
#pragma unroll
  for (int ks = 0; ks < 2; ++ks) { wf[ks][0] = *(const LAS v4u *)(wb_ + ks * 1024); wf[ks][1] = *(const LAS v4u *)(wb_ + (14 + ks) * 1024); }
  SB();
  r0 = f32x4{0.f, 0.f, 0.f, 0.f}; r1 = r0;
  f32x4 ss = r0, gg = r0;
#pragma unroll
  for (int ks = 0; ks < 14; ++ks) {
    if (ks + 2 < 14) {
      wf[(ks + 2) % 3][0] = *(const LAS v4u *)(wb_ + (ks + 2) * 1024);
      wf[(ks + 2) % 3][1] = *(const LAS v4u *)(wb_ + (16 + ks) * 1024);
    }
    SB();
    r0 = mfma32(wf[ks % 3][0], xf[ks], r0);
    r1 = mfma32(wf[ks % 3][1], xf[ks], r1);
    if (MODE == 2) {
      ss = mfma32(v4u{0x3f803f80u, 0x3f803f80u, 0x3f803f80u, 0x3f803f80u}, xf[ks], ss);
      gg = mfma32(xf[ks], xf[ks], gg);
      if (ks == jm) xk = xf[ks];
    }
    SB();
    if (ks >= 7) {
#pragma unroll
      for (int i = LO + ((ks - 7) * (HI - LO)) / 7; i < LO + ((ks - 6) * (HI - LO)) / 7; ++i) pf[i] = LD16C(c.rs, lane * 16, pfb, i * 1024);
    }
  }
  SB();
  if (MODE == 2) lc = ln_coef<true>(ss[0], gram_diag(gg, lane));
}
DI v4u add_res(v4u x, f32x4 a, f32x4 b) {
  v4u o;
  o.x = pk2(bflo(x.x) + rbf(a[0]), bfhi(x.x) + rbf(a[1])); o.y = pk2(bflo(x.y) + rbf(a[2]), bfhi(x.y) + rbf(a[3]));
  o.z = pk2(bflo(x.z) + rbf(b[0]), bfhi(x.z) + rbf(b[1])); o.w = pk2(bflo(x.w) + rbf(b[2]), bfhi(x.w) + rbf(b[3]));
  return o;
}

DI void dual_softmax(f32x4 p0, f32x4 p1, bool v1, v4u &pos, v4u &neg) {
  float pv[8];
#pragma unroll
  for (int i = 0; i < 4; ++i) { pv[i] = rbf(p0[i]) * LOG2E; pv[4 + i] = rbf(p1[i]) * LOG2E; }
  float mx = fmaxf(fmaxf(pv[0], pv[1]), fmaxf(pv[2], pv[3]));
  float mn = fminf(fminf(pv[0], pv[1]), fminf(pv[2], pv[3]));
  const float mx1 = fmaxf(fmaxf(pv[4], pv[5]), fmaxf(pv[6], pv[7]));
  const float mn1 = fminf(fminf(pv[4], pv[5]), fminf(pv[6], pv[7]));
  if (v1) { mx = fmaxf(mx, mx1); mn = fminf(mn, mn1); }
  mx = max_q(mx); mn = min_q(mn);
  float ep[8], en[8];
#pragma unroll
  for (int i = 0; i < 8; ++i) { ep[i] = ex2(pv[i] - mx); en[i] = ex2(mn - pv[i]); }
  float sp = (ep[0] + ep[1]) + (ep[2] + ep[3]), sn = (en[0] + en[1]) + (en[2] + en[3]);
  const float sp1 = (ep[4] + ep[5]) + (ep[6] + ep[7]), sn1 = (en[4] + en[5]) + (en[6] + en[7]);
  if (v1) { sp += sp1; sn += sn1; }
  sp = sum_q(sp); sn = sum_q(sn);
  const float rp = rcp(sp), rn = rcp(sn);
  pos.x = pk2(ep[0] * rp, ep[1] * rp); pos.y = pk2(ep[2] * rp, ep[3] * rp);
  neg.x = pk2(en[0] * rn, en[1] * rn); neg.y = pk2(en[2] * rn, en[3] * rn);
  pos.z = v1 ? pk2(ep[4] * rp, ep[5] * rp) : 0u; pos.w = v1 ? pk2(ep[6] * rp, ep[7] * rp) : 0u;
  neg.z = v1 ? pk2(en[4] * rn, en[5] * rn) : 0u; neg.w = v1 ? pk2(en[6] * rn, en[7] * rn) : 0u;
}
DI v2u trr(const LAS unsigned char *a) {
  return __builtin_bit_cast(v2u, __builtin_amdgcn_ds_read_tr16_b64_v4i16((LAS bf16x4 *)a));
}

__global__ void __launch_bounds__(NTHR, 2) k_main(Params p) {
  extern __shared__ __attribute__((aligned(16))) unsigned char smem[];
  Ctx c;
  c.rs = __builtin_amdgcn_make_buffer_rsrc((void *)p.ws, 0, WS_END, 0x00020000);
  c.ctrl = (gu32 *)p.ws;
  c.L = (LAS unsigned char *)smem;
  c.tid = threadIdx.x; c.lane = c.tid & 63; c.w = __builtin_amdgcn_readfirstlane(c.tid >> 6);
  const int b = blockIdx.x, xcd = b & 7, r = b >> 3;
  const int g = 4 * xcd + (r >> 3), j = r & 7;
  const int tau = 2 * xcd + (r >> 4), head = r & 15;
  const int w = c.w;
  const int jm = r & 15;
  const int jmc = jm < 14 ? jm : 0;
  const f32x4 z4 = {0.f, 0.f, 0.f, 0.f};

  dma_run<84>(p, c, WQKV_T + head * (84 * 1024), L_WQ);
  v4u cpf;
  {
    unsigned long long pc;
    asm volatile("s_getpc_b64 %0" : "=s"(pc));
    const int idx = (r * 8 + w) % CODE_PF;
    cpf = *(const GAS v4u *)((pc & ~255ull) + idx * 1024 + c.lane * 16);
  }

  for (int pair = 0; pair < 2; ++pair) {
    const unsigned e1 = pair + 1;
    const int xin = XIN + pair * ACT_D;
    v4u wr[28];
    v4u phq_p, phq_n;
    {
      FRESH();
      const v4u wph0 = ld16(c.rs, lane * 16, WPHI_T + (head * 2 + 0) * 1024);
      const v4u wph1 = ld16(c.rs, lane * 16, WPHI_T + (head * 2 + 1) * 1024);
      v4u cs[6];
#pragma unroll
      for (int nt = 0; nt < 6; ++nt) cs[nt] = ld16(c.rs, q4 * 16 + nt * 64, CS_QKV + head * 384);
      if (pair > 0) {
        DRAIN();
        wait2(c, C_XN + 2 * tau, C_XN + 2 * tau + 1, 7u * pair);
      }
      STAMP(0);
      v4u xf[14];
      const int xb = opaque(xin + (tau * 8 + w) * TILE_D);
#pragma unroll
      for (int ks = 0; ks < 14; ++ks) xf[ks] = LD16C(c.rs, lane * 16, xb, ks * 1024);
      if (pair == 0) {
        DRAIN();
        __syncthreads();
        asm volatile("" :: "v"(cpf));
      }
      const LAS unsigned char *wb_ = c.L + L_WQ + lane * 16;
      f32x4 ss = z4, gg = z4;
      f32x4 acc[4];
#pragma unroll
      for (int nt = 0; nt < 4; ++nt) acc[nt] = z4;
      {
        v4u wf[2][4];
#pragma unroll
        for (int nt = 0; nt < 4; ++nt) wf[0][nt] = *(const LAS v4u *)(wb_ + ((2 + nt) * 14) * 1024);
        SB();
#pragma unroll
        for (int ks = 0; ks < 14; ++ks) {
          if (ks + 1 < 14) {
#pragma unroll
            for (int nt = 0; nt < 4; ++nt) wf[(ks + 1) & 1][nt] = *(const LAS v4u *)(wb_ + ((2 + nt) * 14 + ks + 1) * 1024);
          }
          SB();
#pragma unroll
          for (int nt = 0; nt < 4; ++nt) acc[nt] = mfma32(wf[ks & 1][nt], xf[ks], acc[nt]);
          ss = mfma32(v4u{0x3f803f80u, 0x3f803f80u, 0x3f803f80u, 0x3f803f80u}, xf[ks], ss);
          gg = mfma32(xf[ks], xf[ks], gg);
          SB();
        }
      }
      STAMP(1);
      const LnC lc = ln_coef<false>(ss[0], gram_diag(gg, lane));
      const bool v1 = q4 < 3;
      {
        v2u kv_[4];
#pragma unroll
        for (int nt = 0; nt < 4; ++nt) {
          const f32x4 v = acc[nt] * lc.A - __builtin_bit_cast(f32x4, cs[2 + nt]) * lc.B;
          kv_[nt] = v2u{pk2(v[0], v[1]), pk2(v[2], v[3])};
        }
        const v4u kp = {kv_[0].x, kv_[0].y, kv_[1].x, kv_[1].y};
        v4u pp, pn;
        dual_softmax(mfma32(wph0, kp, z4), mfma32(wph1, kp, z4), v1, pp, pn);
        LAS unsigned char *pk = c.L + L_PK + (16 * w + t16) * PK_STR + q4 * 8;
        *(LAS v2u *)(pk) = v2u{pp.x, pp.y};
        *(LAS v2u *)(pk + 32) = v2u{pp.z, pp.w};
        *(LAS v2u *)(pk + 64) = v2u{pn.x, pn.y};
        *(LAS v2u *)(pk + 96) = v2u{pn.z, pn.w};
        LAS unsigned char *vv = c.L + L_V + (16 * w + t16) * V_STR + q4 * 8;
        *(LAS v2u *)(vv) = kv_[2];
        *(LAS v2u *)(vv + 32) = v1 ? kv_[3] : v2u{0x00003f80u, 0u};
      }
      __syncthreads();
      STAMP(2);
      {
        const int ft = w >> 1, dt = w & 1;
        const int rq = (lane & 15) >> 2, rp = lane & 3;
        const LAS unsigned char *pa = c.L + L_PK + (8 * q4 + rq) * PK_STR + (16 * ft + 4 * rp) * 2;
        const LAS unsigned char *pb = c.L + L_V + (8 * q4 + rq) * V_STR + (16 * dt + 4 * rp) * 2;
        f32x4 kv = z4;
#pragma unroll
        for (int ks = 0; ks < 4; ++ks) {
          const v2u a0 = trr(pa + (32 * ks) * PK_STR), a1 = trr(pa + (32 * ks + 4) * PK_STR);
          const v2u b0 = trr(pb + (32 * ks) * V_STR), b1 = trr(pb + (32 * ks + 4) * V_STR);
          kv = mfma32(v4u{a0.x, a0.y, a1.x, a1.y}, v4u{b0.x, b0.y, b1.x, b1.y}, kv);
        }
        st8(c.rs, lane * 16 + (tau & 1) * 8, KVP_B + ((pair * 16 + head) * 8 + (tau >> 1)) * 8192 + w * 1024, v2u{pk2(kv[0], kv[1]), pk2(kv[2], kv[3])});
      }
      STAMP(3);
      publish(c, C_KV + head);
      STAMP(4);
      dma_run<28>(p, c, WO_T + jmc * (28 * 1024), L_WO);
      {
        f32x4 q0 = z4, q1 = z4;
        v4u wf[3][2];
#pragma unroll
        for (int ks = 0; ks < 2; ++ks) { wf[ks][0] = *(const LAS v4u *)(wb_ + ks * 1024); wf[ks][1] = *(const LAS v4u *)(wb_ + (14 + ks) * 1024); }
        SB();
#pragma unroll
        for (int ks = 0; ks < 14; ++ks) {
          if (ks + 2 < 14) {
            wf[(ks + 2) % 3][0] = *(const LAS v4u *)(wb_ + (ks + 2) * 1024);
            wf[(ks + 2) % 3][1] = *(const LAS v4u *)(wb_ + (16 + ks) * 1024);
          }
          SB();
          q0 = mfma32(wf[ks % 3][0], xf[ks], q0);
          q1 = mfma32(wf[ks % 3][1], xf[ks], q1);
          SB();
        }
        q0 = q0 * lc.A - __builtin_bit_cast(f32x4, cs[0]) * lc.B;
        q1 = q1 * lc.A - __builtin_bit_cast(f32x4, cs[1]) * lc.B;
        const v4u qp = {pk2(q0[0], q0[1]), pk2(q0[2], q0[3]), pk2(q1[0], q1[1]), pk2(q1[2], q1[3])};
        dual_softmax(mfma32(wph0, qp, z4), mfma32(wph1, qp, z4), v1, phq_p, phq_n);
      }
    }
    {
      FRESH();
      wait1(c, C_KV + head, 16u * e1);
      STAMP(5);
      v4u pr[8];
      const int kb = opaque(KVP_B + (pair * 16 + head) * 8 * 8192 + w * 1024);
#pragma unroll
      for (int t = 0; t < 8; ++t) pr[t] = ld16(c.rs, lane * 16, kb + t * 8192);
      SB();
      f32x4 s = z4;
#pragma unroll
      for (int t = 0; t < 8; ++t) {
        s += f32x4{bflo(pr[t].x), bfhi(pr[t].x), bflo(pr[t].y), bfhi(pr[t].y)};
        s += f32x4{bflo(pr[t].z), bfhi(pr[t].z), bflo(pr[t].w), bfhi(pr[t].w)};
      }
      {
        const int ft = w >> 1, dt = w & 1, d = 16 * dt + t16, part = ft >> 1, half = ft & 1;
        *(LAS v2u *)(c.L + L_KVS + ((d * 2 + part) * 4 + q4) * 16 + half * 8) = v2u{pk2(s[0], s[1]), pk2(s[2], s[3])};
      }
      __syncthreads();
      f32x4 nm[2];
#pragma unroll
      for (int dt = 0; dt < 2; ++dt) {
        const v4u ap = *(const LAS v4u *)(c.L + L_KVS + (((16 * dt + t16) * 2 + 0) * 4 + q4) * 16);
        const v4u an = *(const LAS v4u *)(c.L + L_KVS + (((16 * dt + t16) * 2 + 1) * 4 + q4) * 16);
        nm[dt] = mfma32(ap, phq_p, z4);
        nm[dt] = mfma32(an, phq_n, nm[dt]);
      }
      const float den0 = __shfl(nm[1][0], 48 + t16);
      const float rden = rcp(rbf(rbf(den0) + 1e-6f));
#pragma unroll
      for (int dt = 0; dt < 2; ++dt) {
        if (dt == 0 || q4 < 3) {
          const int col = DH * head + 16 * dt + 4 * q4;
          v2u o;
          o.x = pk2(rbf(nm[dt][0]) * rden, rbf(nm[dt][1]) * rden);
          o.y = pk2(rbf(nm[dt][2]) * rden, rbf(nm[dt][3]) * rden);
          st8(c.rs, (col >> 3) * 256 + t16 * 16 + ((col >> 2) & 1) * 8, O_B + pair * ACT_D + (tau * 8 + w) * TILE_D, o);
        }
      }
      STAMP(6);
      publish(c, C_O + tau);
      STAMP(7);
      dma_run<28>(p, c, W1_T + jmc * (28 * 1024), L_W1);
      dma_run<28>(p, c, W2_T + jmc * (28 * 1024), L_W2);
    }
    const int w3b = W3_T + (16 * (j < 7 ? j : 0) + 2 * w) * 14 * 1024;
    if (jm < 14) {
      const int tile = tau * 8 + w;
      v4u yk;
      {
        FRESH();
        const int eo = tile * TILE_D + jm * 1024;
        const v4u xr = ld16(c.rs, lane * 16, xin + eo);
        wait1(c, C_O + tau, 16u * e1); STAMP(8);
        f32x4 r0, r1; LnC lc; v4u xk;
        gemm_dir<0, 0, 0>(c, O_B + pair * ACT_D, tile, L_WO, jm, r0, r1, lc, xk, w3b, wr);
        STAMP(9);
        st16(c.rs, lane * 16, XP_B + pair * ACT_D + eo, add_res(xr, r0, r1));
        STAMP(10); publish(c, C_XP + tau); STAMP(11);
        load_run<0, 10, 28>(c, w3b, wr);
      }
      {
        FRESH();
        const int eo = tile * TILE_D + jm * 1024;
        const f32x4 cs0 = __builtin_bit_cast(f32x4, ld16(c.rs, q4 * 32, CS_W1 + jm * 128));
        const f32x4 cs1 = __builtin_bit_cast(f32x4, ld16(c.rs, q4 * 32 + 16, CS_W1 + jm * 128));
        wait1(c, C_XP + tau, 14u * e1); STAMP(12);
        f32x4 r0, r1; LnC lc; v4u xk;
        gemm_dir<2, 0, 0>(c, XP_B + pair * ACT_D, tile, L_W1, jm, r0, r1, lc, xk, w3b, wr);
        STAMP(13);
        r0 = r0 * lc.A - cs0 * lc.B;
        r1 = r1 * lc.A - cs1 * lc.B;
        {
          const int k8 = 4 * jm + q4, cg = k8 / 14;
          const int d = (cg == 0) ? 1 : (cg == 1) ? -1 : (cg == 2) ? 2 : -2;
          const int tok = 16 * tile + t16, sl = tok + d;
          const int ho = (sl < 0 || sl >= S_) ? 0x7ff00000 : (sl >> 4) * TILE_D + k8 * 256 + (sl & 15) * 16;
          st16(c.rs, ho, H_B + pair * ACT_D, v4u{gelu2(r0[0], r0[1]), gelu2(r0[2], r0[3]), gelu2(r1[0], r1[1]), gelu2(r1[2], r1[3])});
          const int src = tok - d;
          if (src < 0 || src >= S_) st16(c.rs, (tok >> 4) * TILE_D + k8 * 256 + (tok & 15) * 16, H_B + pair * ACT_D, v4u{0u, 0u, 0u, 0u});
        }
#pragma unroll
        for (int e = 0; e < 4; ++e) {
          const unsigned d = pk2(bflo(xk[e]) - lc.mb, bfhi(xk[e]) - lc.mb);
          yk[e] = pk2(bflo(d) * lc.r, bfhi(d) * lc.r);
        }
        STAMP(14); publish(c, C_H + tau); STAMP(15);
        load_run<10, 19, 28>(c, w3b, wr);
      }
      {
        FRESH();
        const int eo = tile * TILE_D + jm * 1024;
        wait3(c, C_H + tau, C_H + (tau > 0 ? tau - 1 : tau), C_H + (tau < 15 ? tau + 1 : tau), 14u * e1); STAMP(16);
        f32x4 r0, r1; LnC lc; v4u xk;
        gemm_dir<0, 0, 0>(c, H_B + pair * ACT_D, tile, L_W2, jm, r0, r1, lc, xk, w3b, wr);
        STAMP(17);
        st16(c.rs, lane * 16, X1_B + pair * ACT_D + eo, add_res(yk, r0, r1));
        STAMP(18); publish(c, C_X1 + tau); STAMP(19);
        load_run<19, 28, 28>(c, w3b, wr);
      }
    } else {
      FRESH();
      load_run<0, 28, 28>(c, w3b, wr);
    }
    if (j < 7) {
      {
        FRESH();
        const f32x4 cs0 = __builtin_bit_cast(f32x4, ld16(c.rs, q4 * 32, CS_W3 + (256 * j + 32 * w) * 4));
        const f32x4 cs1 = __builtin_bit_cast(f32x4, ld16(c.rs, q4 * 32 + 16, CS_W3 + (256 * j + 32 * w) * 4));
        wait1(c, C_X1 + (g >> 1), 14u * e1); STAMP(20);
        {
          v4u a[7];
          stage_issue<0>(c, X1_B + pair * ACT_D, g, a);
          stage_finish<1>(c, j, a); STAMP(21);
        }
        f32x4 acc[4][2];
#pragma unroll
        for (int m = 0; m < 4; ++m) { acc[m][0] = z4; acc[m][1] = z4; }
        const int w4b = opaque(W4_T + (4 * j * 56 + 7 * w) * 1024);
        {
          v4u bf[2][4];
          const LAS unsigned char *ab = c.L + L_A + lane * 16;
#pragma unroll
          for (int m = 0; m < 4; ++m) bf[0][m] = *(const LAS v4u *)(ab + m * TILE_D);
#pragma unroll
          for (int ks = 0; ks < 14; ++ks) {
            if (ks + 1 < 14) {
#pragma unroll
              for (int m = 0; m < 4; ++m) bf[(ks + 1) & 1][m] = *(const LAS v4u *)(ab + m * TILE_D + (ks + 1) * 1024);
            }
            SB();
#pragma unroll
            for (int m = 0; m < 4; ++m) {
              acc[m][0] = mfma32(wr[ks], bf[ks & 1][m], acc[m][0]);
              acc[m][1] = mfma32(wr[14 + ks], bf[ks & 1][m], acc[m][1]);
            }
            SB();
            wr[ks] = LD16C(c.rs, lane * 16, w4b, ((ks / 7) * 56 + ks % 7) * 1024);
            wr[14 + ks] = LD16C(c.rs, lane * 16, w4b, ((2 + ks / 7) * 56 + ks % 7) * 1024);
          }
          SB();
        }
#pragma unroll
        for (int m = 0; m < 4; ++m) {
          const f32x2 st = *(const LAS f32x2 *)(c.L + L_ST + (16 * m + t16) * 8);
          const f32x4 u0 = acc[m][0] * st.x - cs0 * st.y, u1 = acc[m][1] * st.x - cs1 * st.y;
          const v4u o = {gelu2(u0[0], u0[1]), gelu2(u0[2], u0[3]), gelu2(u1[0], u1[1]), gelu2(u1[2], u1[3])};
          st16(c.rs, lane * 16, G_B + pair * ACT_F + (4 * g + m) * TILE_F + (32 * j + 4 * w) * 256, o);
        }
        STAMP(22); publish(c, C_G + g); STAMP(23);
      }
      {
        FRESH();
        const int roff = q4 * TILE_D + t16 * 16;
        const int rso = 4 * g * TILE_D + (8 * j + w) * 256;
        const v4u rxp = ld16(c.rs, roff, XP_B + pair * ACT_D + rso);
        const v4u rx1 = ld16(c.rs, roff, X1_B + pair * ACT_D + rso);
        wait1(c, C_G + g, 7u * e1); STAMP(24);
        const int abase = opaque(G_B + pair * ACT_F + 4 * g * TILE_F + 7 * w * 1024);
        constexpr int RING = 14;
        v4u a[RING];
#pragma unroll
        for (int i = 0; i < RING; ++i) a[i] = LD16C(c.rs, lane * 16, abase, (i / 7) * TILE_F + (i % 7) * 1024);
        SB();
#pragma unroll
        for (int m = 0; m < 4; ++m) {
          f32x4 acc[4];
#pragma unroll
          for (int t = 0; t < 4; ++t) acc[t] = z4;
#pragma unroll
          for (int kk = 0; kk < 7; ++kk) {
            const int i = m * 7 + kk;
            const v4u cur = a[i % RING];
            if (i + RING < 28) a[i % RING] = LD16C(c.rs, lane * 16, abase, ((i + RING) / 7) * TILE_F + ((i + RING) % 7) * 1024);
#pragma unroll
            for (int t = 0; t < 4; ++t) acc[t] = mfma32(wr[t * 7 + kk], cur, acc[t]);
            SB();
          }
#pragma unroll
          for (int t = 0; t < 4; ++t) *(LAS f32x4 *)(c.L + L_RED + ((w * 16 + m * 4 + t) * 64 + lane) * 16) = acc[t];
        }
        if (pair == 0) {
          const int qb = opaque(WQKV_T + head * (84 * 1024) + w * 1024);
#pragma unroll
          for (int i = 0; i < 11; ++i) wr[i] = ld16(c.rs, lane * 16, qb + (w + 8 * i < 84 ? i * 8192 : 0));
          SB();
        }
        STAMP(25);
        __syncthreads();
        f32x4 s0 = z4, s1 = z4;
        {
          const int t = w >> 1, qq = 2 * (w & 1);
#pragma unroll
          for (int ww = 0; ww < 8; ++ww) {
            s0 += *(const LAS f32x4 *)(c.L + L_RED + ((ww * 16 + q4 * 4 + t) * 64 + t16 + 16 * qq) * 16);
            s1 += *(const LAS f32x4 *)(c.L + L_RED + ((ww * 16 + q4 * 4 + t) * 64 + t16 + 16 * (qq + 1)) * 16);
          }
        }
        v4u o;
        o.x = pk2(bflo(rxp.x) + rbf(bflo(rx1.x) + rbf(s0[0])), bfhi(rxp.x) + rbf(bfhi(rx1.x) + rbf(s0[1])));
        o.y = pk2(bflo(rxp.y) + rbf(bflo(rx1.y) + rbf(s0[2])), bfhi(rxp.y) + rbf(bfhi(rx1.y) + rbf(s0[3])));
        o.z = pk2(bflo(rxp.z) + rbf(bflo(rx1.z) + rbf(s1[0])), bfhi(rxp.z) + rbf(bfhi(rx1.z) + rbf(s1[1])));
        o.w = pk2(bflo(rxp.w) + rbf(bflo(rx1.w) + rbf(s1[2])), bfhi(rxp.w) + rbf(bfhi(rx1.w) + rbf(s1[3])));
        STAMP(26);
        if (pair == 0) {
          st16(c.rs, roff, XIN + ACT_D + rso, o);
          publish(c, C_XN + g);
#pragma unroll
          for (int i = 0; i < 11; ++i)
            if (w + 8 * i < 84) *(LAS v4u *)(c.L + L_WQ + (w + 8 * i) * 1024 + lane * 16) = wr[i];
        } else {
          *(v4u *)(p.out + (size_t)(64 * g + 16 * q4 + t16) * D_ + 64 * j + 8 * w) = o;
        }
        STAMP(27);
      }
    }
    else if (pair == 0) {
      DRAIN();
      __syncthreads();
      c.lane = opaque_v(c.lane);
      dma_run<84>(p, c, WQKV_T + head * (84 * 1024), L_WQ);
    }
  }
}

}

extern "C" void kernel_launch(void *const *d_in, const int *in_sizes, int n_in, void *d_out, int out_size, void *d_ws,
                              size_t ws_size, hipStream_t stream) {
  Params p;
  p.x = (const unsigned short *)d_in[0];
  p.wqkv = (const unsigned short *)d_in[1];
  p.wphi = (const unsigned short *)d_in[2];
  p.wo = (const unsigned short *)d_in[3];
  p.w1 = (const unsigned short *)d_in[5];
  p.w2 = (const unsigned short *)d_in[6];
  p.w3 = (const unsigned short *)d_in[7];
  p.w4 = (const unsigned short *)d_in[8];
  p.out = (unsigned short *)d_out;
  p.ws = (unsigned char *)d_ws;
  (void)hipFuncSetAttribute((const void *)k_main, hipFuncAttributeMaxDynamicSharedMemorySize, LDS_BYTES);
  hipLaunchKernelGGL(k_prep, dim3(PREP_BLOCKS), dim3(256), 0, stream, p);
  hipLaunchKernelGGL(k_main, dim3(256), dim3(NTHR), LDS_BYTES, stream, p);
}
```

```cpp
#include <hip/hip_runtime.h>
#include <hip/hip_bf16.h>
#include <stdint.h>

#define GAS __attribute__((address_space(1)))
#define LAS __attribute__((address_space(3)))
#define RLX_AGENT __ATOMIC_RELAXED, __HIP_MEMORY_SCOPE_AGENT
#define DI __device__ __forceinline__
typedef unsigned v4u __attribute__((ext_vector_type(4)));
typedef unsigned v2u __attribute__((ext_vector_type(2)));
typedef float f32x4 __attribute__((ext_vector_type(4)));
typedef float f32x2 __attribute__((ext_vector_type(2)));
typedef short bf16x8 __attribute__((ext_vector_type(8)));
typedef short bf16x4 __attribute__((ext_vector_type(4)));
typedef __bf16 bfv2 __attribute__((ext_vector_type(2)));
typedef GAS unsigned gu32;

namespace {

constexpr int S_ = 2048, D_ = 448, DH = 28, F_ = 1792;
constexpr int NTHR = 512;
constexpr int TILE_D = (D_ / 8) * 256;
constexpr int TILE_F = (F_ / 8) * 256;
constexpr int ACT_D = 128 * TILE_D;
constexpr int ACT_F = 128 * TILE_F;

constexpr int CSTR = 64;
constexpr int C_XN = 1;
constexpr int C_KV = C_XN + 32;
constexpr int C_O = C_KV + 16;
constexpr int C_XP = C_O + 16;
constexpr int C_H = C_XP + 16;
constexpr int C_X1 = C_H + 16;
constexpr int C_G = C_X1 + 16;
constexpr int C_NCTR = C_G + 32;
constexpr int CTRL_BYTES = ((C_NCTR * CSTR * 4 + 65535) / 65536) * 65536;
static_assert(C_NCTR * CSTR * 4 <= CTRL_BYTES, "control block");
constexpr int WQKV_T = CTRL_BYTES;
constexpr int WO_T = WQKV_T + 96 * 14 * 1024;
constexpr int W1_T = WO_T + 28 * 14 * 1024;
constexpr int W2_T = W1_T + 28 * 14 * 1024;
constexpr int W3_T = W2_T + 28 * 14 * 1024;
constexpr int W4_T = W3_T + 112 * 14 * 1024;
constexpr int WPHI_T = W4_T + 28 * 56 * 1024;
constexpr int CS_QKV = WPHI_T + 16 * 2 * 1024;
constexpr int CS_W1 = CS_QKV + 96 * 16 * 4;
constexpr int CS_W3 = CS_W1 + 448 * 4;
constexpr int W_END = CS_W3 + 1792 * 4;
constexpr int XIN = (W_END + 4095) & ~4095;
constexpr int O_B = XIN + 2 * ACT_D;
constexpr int XP_B = O_B + ACT_D;
constexpr int H_B = XP_B + ACT_D;
constexpr int X1_B = H_B + ACT_D;
constexpr int G_B = X1_B + ACT_D;
constexpr int KVP_B = G_B + ACT_F;
constexpr int WS_END = KVP_B + 2 * 16 * 16 * 8192;

constexpr int L_A = 0;
constexpr int L_ST = 57344;
constexpr int L_RED = 0;
constexpr int L_WQ = 65536;
constexpr int PK_STR = 136, V_STR = 72;
constexpr int L_PK = 0;
constexpr int L_V = 17408;
constexpr int L_KVS = 26624;
constexpr int L_WO = 32768;
constexpr int L_W1 = 65536;
constexpr int L_W2 = 94208;
constexpr int LDS_BYTES = 151552;

constexpr int CODE_PF = 18;
constexpr unsigned kSpinCap = 1u << 22;

struct Params {
  const unsigned short *x, *wqkv, *wphi, *wo, *w1, *w2, *w3, *w4;
  unsigned short *out;
  unsigned char *ws;
};

DI float asf(unsigned u) { return __builtin_bit_cast(float, u); }
DI unsigned asu(float f) { return __builtin_bit_cast(unsigned, f); }
DI float bflo(unsigned u) { return asf(u << 16); }
DI float bfhi(unsigned u) { return asf(u & 0xffff0000u); }
DI unsigned pk2(float a, float b) {
  f32x2 v = {a, b};
  return __builtin_bit_cast(unsigned, __builtin_convertvector(v, bfv2));
}
DI float rbf(float a) { return (float)(__bf16)a; }
DI float rcp(float a) { return __builtin_amdgcn_rcpf(a); }
DI float ex2(float a) { return __builtin_amdgcn_exp2f(a); }
DI float dot2(unsigned a, unsigned b, float c) {
  return __builtin_amdgcn_fdot2_f32_bf16(__builtin_bit_cast(bfv2, a), __builtin_bit_cast(bfv2, b), c, false);
}
DI f32x4 mfma32(v4u a, v4u b, f32x4 c) {
  return __builtin_amdgcn_mfma_f32_16x16x32_bf16(__builtin_bit_cast(bf16x8, a), __builtin_bit_cast(bf16x8, b), c, 0, 0, 0);
}
#define DRAIN() asm volatile("s_waitcnt vmcnt(0)" ::: "memory")
constexpr int SC1 = 16;
DI v4u ld16(__amdgpu_buffer_rsrc_t rs, int voff, int soff) { return __builtin_amdgcn_raw_buffer_load_b128(rs, voff, soff, SC1); }
DI v2u ld8(__amdgpu_buffer_rsrc_t rs, int voff, int soff) { return __builtin_amdgcn_raw_buffer_load_b64(rs, voff, soff, SC1); }
DI void st16(__amdgpu_buffer_rsrc_t rs, int voff, int soff, v4u v) { __builtin_amdgcn_raw_buffer_store_b128(v, rs, voff + soff, 0, SC1); }
DI void st8(__amdgpu_buffer_rsrc_t rs, int voff, int soff, v2u v) { __builtin_amdgcn_raw_buffer_store_b64(v, rs, voff, soff, SC1); }
DI void st4(__amdgpu_buffer_rsrc_t rs, int voff, int soff, unsigned v) { __builtin_amdgcn_raw_buffer_store_b32(v, rs, voff, soff, SC1); }
#define LD16C(rs, voff, soff, co) ld16(rs, (voff) + ((co) & 4095), (soff) + ((co) & ~4095))
DI int opaque(int x) { asm volatile("" : "+s"(x)); return x; }
DI int opaque_v(int x) { asm volatile("" : "+v"(x)); return x; }
#define FRESH() c.lane = opaque_v(c.lane); const int lane = c.lane, t16 = lane & 15, q4 = lane >> 4; (void)t16; (void)q4;
#define SB() __builtin_amdgcn_sched_barrier(0)

template <int CTRL> DI float dppf(float v) {
  return asf(__builtin_amdgcn_update_dpp(0, (int)asu(v), CTRL, 0xf, 0xf, false));
}
DI float sum_q(float v) {
  auto r = __builtin_amdgcn_permlane16_swap(asu(v), asu(v), false, false);
  v = asf(r[0]) + asf(r[1]);
  auto s = __builtin_amdgcn_permlane32_swap(asu(v), asu(v), false, false);
  return asf(s[0]) + asf(s[1]);
}
DI float max_q(float v) {
  auto r = __builtin_amdgcn_permlane16_swap(asu(v), asu(v), false, false);
  v = fmaxf(asf(r[0]), asf(r[1]));
  auto s = __builtin_amdgcn_permlane32_swap(asu(v), asu(v), false, false);
  return fmaxf(asf(s[0]), asf(s[1]));
}
DI float min_q(float v) {
  auto r = __builtin_amdgcn_permlane16_swap(asu(v), asu(v), false, false);
  v = fminf(asf(r[0]), asf(r[1]));
  auto s = __builtin_amdgcn_permlane32_swap(asu(v), asu(v), false, false);
  return fminf(asf(s[0]), asf(s[1]));
}

constexpr float LOG2E = 1.4426950408889634f;
DI unsigned gelu2(float a, float b) {
  const f32x2 x = {a, b};
  constexpr float c0 = -2.0f * 0.7978845608028654f * LOG2E, c1 = c0 * 0.044715f;
  const f32x2 u = x * (x * x * c1 + c0);
  f32x2 e = {ex2(u.x), ex2(u.y)};
  e = e + 1.0f;
  const f32x2 r = {rcp(e.x), rcp(e.y)};
  const f32x2 o = x * r;
  return pk2(o.x, o.y);
}

DI void poll_ge(gu32 *w, unsigned want, gu32 *tmo) {
#pragma nounroll
  for (unsigned spins = 0;;) {
    if (__hip_atomic_load(w, RLX_AGENT) >= want) return;
    __builtin_amdgcn_s_sleep(1);
    if (++spins > kSpinCap) { __hip_atomic_store(tmo, 1u, RLX_AGENT); return; }
    if ((spins & 1023u) == 0u && __hip_atomic_load(tmo, RLX_AGENT) != 0u) return;
  }
}

struct LnC { float A, B, mb, r; };
template <bool TWICE> DI LnC ln_coef(float s1, float s2) {
  const float mean = s1 * (1.0f / D_);
  const float var = fmaxf(s2 * (1.0f / D_) - mean * mean, 0.f);
  LnC o;
  o.mb = rbf(mean);
  const float sd = rbf(__builtin_amdgcn_sqrtf(rbf(rbf(var) + 1e-5f)));
  o.r = rcp(sd);
  if (TWICE) {
    const float my = (mean - o.mb) * o.r, vy = var * o.r * o.r;
    const float mby = rbf(my);
    const float ry = rcp(rbf(__builtin_amdgcn_sqrtf(rbf(rbf(vy) + 1e-5f))));
    o.A = o.r * ry;
    o.B = (o.mb * o.r + mby) * ry;
  } else {
    o.A = o.r;
    o.B = o.mb * o.r;
  }
  return o;
}

DI float gram_diag(f32x4 gg, int lane) {
  const int t16 = lane & 15, q4 = lane >> 4;
  const float a = (t16 & 1) ? gg[1] : gg[0], b = (t16 & 1) ? gg[3] : gg[2];
  const float d = (t16 & 2) ? b : a;
  return sum_q(q4 == (t16 >> 2) ? d : 0.f);
}

constexpr int NB_X = S_ * (D_ / 8) / 256;
constexpr int NJ_Q = 3 * 4 * 14;
constexpr int NJ_S = 3 * 7 * 14, NJ_3 = 28 * 14, NJ_4 = 7 * 56;
constexpr int NB_W = NJ_Q + NJ_S + NJ_3 + NJ_4;
constexpr int NB_PHI = 8;
constexpr int NB_CS = 21 + 7 + 28;
constexpr int PREP_BLOCKS = NB_W + NB_CS + NB_PHI + NB_X;

__global__ void __launch_bounds__(256) k_prep(Params p) {
  __shared__ __attribute__((aligned(16))) unsigned char sm[32 * 240];
  f32x4 *red = (f32x4 *)sm;
  const __amdgpu_buffer_rsrc_t rs = __builtin_amdgcn_make_buffer_rsrc((void *)p.ws, 0, WS_END, 0x00020000);
  const int tid = threadIdx.x;
  int b = blockIdx.x < NB_CS + NB_PHI ? NB_W + blockIdx.x : (blockIdx.x < NB_CS + NB_PHI + NB_W ? blockIdx.x - (NB_CS + NB_PHI) : blockIdx.x);
  if (b < NJ_Q) {
    const int ks = b % 14, hg = (b / 14) & 3, part = b / 56;
    const unsigned short *src = p.wqkv + (size_t)(ks * 32) * (3 * D_) + part * D_ + hg * 112;
#pragma unroll
    for (int i = 0; i < 2; ++i) {
      const int ch = tid + 256 * i;
      if (ch < 448) {
        const int row = ch / 14, cc = ch % 14;
        *(v4u *)(sm + row * 240 + cc * 16) = *(const v4u *)(src + (size_t)row * (3 * D_) + cc * 8);
      }
    }
    __syncthreads();
#pragma unroll
    for (int i = 0; i < 2; ++i) {
      const int f = (tid >> 6) + 4 * i, slot = tid & 63, r = slot & 15, kg = slot >> 4;
      const int cl = 16 * (f & 1) + r;
      const bool valid = cl < DH;
      const unsigned short *q_ = (const unsigned short *)(sm + (8 * kg) * 240) + 28 * (f >> 1) + (valid ? cl : 0);
      v4u o;
#pragma unroll
      for (int jj = 0; jj < 4; ++jj) o[jj] = valid ? (unsigned)q_[(2 * jj) * 120] | ((unsigned)q_[(2 * jj + 1) * 120] << 16) : 0u;
      const int nt = (4 * hg + (f >> 1)) * 6 + part * 2 + (f & 1);
      st16(rs, WQKV_T + (nt * 14 + ks) * 1024 + slot * 16, 0, o);
    }
    return;
  }
  if (b < NB_W) {
    int jb = b - NJ_Q;
    const unsigned short *src; int ld, dst, cb, ks, KS; bool perm = true;
    if (jb < NJ_S) {
      const int m = jb / 98, rr = jb % 98;
      cb = rr / 14; ks = rr % 14; KS = 14;
      src = m == 0 ? p.wo : (m == 1 ? p.w1 : p.w2); ld = D_; dst = WO_T + m * (28 * 14 * 1024);
    } else if (jb < NJ_S + NJ_3) {
      jb -= NJ_S; cb = jb / 14; ks = jb % 14; KS = 14; src = p.w3; ld = F_; dst = W3_T;
    } else {
      jb -= NJ_S + NJ_3; cb = jb / 56; ks = jb % 56; KS = 56; src = p.w4; ld = D_; dst = W4_T; perm = false;
    }
    {
      const int row = tid >> 3, cc = tid & 7;
      *(v4u *)(sm + row * 144 + cc * 16) = *(const v4u *)(src + (size_t)(ks * 32 + row) * ld + cb * 64 + cc * 8);
    }
    __syncthreads();
    const int f = tid >> 6, slot = tid & 63, r = slot & 15, kg = slot >> 4;
    const int cl = perm ? 32 * (f >> 1) + 8 * (r >> 2) + 4 * (f & 1) + (r & 3) : 16 * f + r;
    const unsigned short *q_ = (const unsigned short *)(sm + (8 * kg) * 144) + cl;
    v4u o;
#pragma unroll
    for (int jj = 0; jj < 4; ++jj) o[jj] = (unsigned)q_[(2 * jj) * 72] | ((unsigned)q_[(2 * jj + 1) * 72] << 16);
    st16(rs, dst + ((4 * cb + f) * KS + ks) * 1024 + slot * 16, 0, o);
    return;
  }
  b -= NB_W;
  if (b < NB_CS) {
    const unsigned short *src; int ld, col0;
    if (b < 21) { src = p.wqkv; ld = 3 * D_; col0 = b * 64; }
    else if (b < 28) { src = p.w1; ld = D_; col0 = (b - 21) * 64; }
    else { src = p.w3; ld = F_; col0 = (b - 28) * 64; }
    const int n4 = tid & 15, ksl = tid >> 4;
    const unsigned short *s0 = src + (size_t)(ksl * 28) * ld + col0 + 4 * n4;
    f32x4 s = {0.f, 0.f, 0.f, 0.f};
    v2u e[28];
#pragma unroll
    for (int k = 0; k < 28; ++k) e[k] = *(const v2u *)(s0 + (size_t)k * ld);
    SB();
#pragma unroll
    for (int k = 0; k < 28; ++k) s += f32x4{bflo(e[k].x), bfhi(e[k].x), bflo(e[k].y), bfhi(e[k].y)};
    red[tid] = s;
    __syncthreads();
    if (tid < 64) {
      float a = 0.f;
#pragma unroll
      for (int k = 0; k < 16; ++k) a += ((const float *)red)[(k * 16 + (tid >> 2)) * 4 + (tid & 3)];
      const int col = col0 + tid;
      int off;
      if (b < 21) {
        const int part = col / D_, rem = col % D_, head = rem / DH, cc = rem % DH;
        off = CS_QKV + ((head * 6 + part * 2 + (cc >> 4)) * 16 + (cc & 15)) * 4;
      } else if (b < 28) off = CS_W1 + col * 4;
      else off = CS_W3 + col * 4;
      st4(rs, off, 0, asu(a));
    } else if (b == 0 && tid >= 64) {
      const int i = tid - 64;
      if (i < 192) st4(rs, CS_QKV + (((i >> 2) * 2 + 1) * 16 + 12 + (i & 3)) * 4, 0, 0u);
    }
    return;
  }
  b -= NB_CS;
  if (b < NB_PHI) {
    const int pid = b * 256 + tid, fr = pid >> 6, lane = pid & 63, r = lane & 15, kg = lane >> 4;
    const int h = fr >> 1, f = (fr & 1) * 16 + r;
    unsigned short e[8];
#pragma unroll
    for (int j = 0; j < 8; ++j) {
      const int d = (j < 4) ? 4 * kg + j : 16 + 4 * kg + (j - 4);
      e[j] = (d < DH && f < DH) ? p.wphi[(h * DH + d) * DH + f] : (unsigned short)0;
    }
    v4u v;
    v.x = e[0] | ((unsigned)e[1] << 16); v.y = e[2] | ((unsigned)e[3] << 16);
    v.z = e[4] | ((unsigned)e[5] << 16); v.w = e[6] | ((unsigned)e[7] << 16);
    st16(rs, WPHI_T + pid * 16, 0, v);
    if (pid < C_NCTR) __hip_atomic_store((gu32 *)p.ws + CSTR * pid, 0u, RLX_AGENT);
    return;
  }
  b -= NB_PHI;
  {
    const int fr = b * 4 + (tid >> 6), tile = fr / 14, ks = fr % 14, lane = tid & 63;
    const v4u v = *(const v4u *)(p.x + (size_t)(tile * 16 + (lane & 15)) * D_ + ks * 32 + (lane >> 4) * 8);
    st16(rs, XIN + tile * TILE_D + ks * 1024 + lane * 16, 0, v);
  }
}

struct Ctx {
  __amdgpu_buffer_rsrc_t rs;
  gu32 *ctrl;
  LAS unsigned char *L;
  int tid, lane, w;
};

DI void wait1(const Ctx &c, int ctr, unsigned want) {
  if (c.tid == NTHR - 64) {
    poll_ge(c.ctrl + CSTR * ctr, want, c.ctrl);
    __builtin_amdgcn_fence(__ATOMIC_ACQUIRE, "wavefront");
  }
  __syncthreads();
}
DI void wait2(const Ctx &c, int c0, int c1, unsigned want) {
  if (c.tid >= NTHR - 64 && c.tid < NTHR - 62) {
    poll_ge(c.ctrl + CSTR * (c.tid == NTHR - 64 ? c0 : c1), want, c.ctrl);
    __builtin_amdgcn_fence(__ATOMIC_ACQUIRE, "wavefront");
  }
  __syncthreads();
}
DI void wait3(const Ctx &c, int c0, int c1, int c2, unsigned want) {
  if (c.tid >= NTHR - 64 && c.tid < NTHR - 61) {
    poll_ge(c.ctrl + CSTR * (c.tid == NTHR - 64 ? c0 : c.tid == NTHR - 63 ? c1 : c2), want, c.ctrl);
    __builtin_amdgcn_fence(__ATOMIC_ACQUIRE, "wavefront");
  }
  __syncthreads();
}
DI void publish(const Ctx &c, int ctr) {
  DRAIN();
  __syncthreads();
  if (c.tid == 0) __hip_atomic_fetch_add(c.ctrl + CSTR * ctr, 1u, RLX_AGENT);
}

DI void dma1k(const unsigned char *gsrc, unsigned lds_dst) {
  unsigned keep;
  asm volatile("s_mov_b32 %0, m0\n\ts_mov_b32 m0, %2\n\ts_nop 0\n\tglobal_load_lds_dwordx4 %1, off sc1\n\ts_mov_b32 m0, %0"
               : "=&s"(keep) : "v"(gsrc), "s"(lds_dst) : "memory");
}
template <int NFR> DI void dma_run(const Params &p, const Ctx &c, int src, int dst) {
  const unsigned char *g = p.ws + src + c.w * 1024 + c.lane * 16;
  const unsigned l = (unsigned)(size_t)c.L + dst + c.w * 1024;
#pragma unroll
  for (int i = 0; i < (NFR + 7) / 8; ++i) {
    if (c.w + 8 * i < NFR) dma1k(g + i * 8192, l + i * 8192);
  }
}

template <int LO, int HI, int N> DI void load_run(const Ctx &c, int base, v4u (&w)[N]) {
  base = opaque(base);
#pragma unroll
  for (int i = LO; i < HI; ++i) w[i] = LD16C(c.rs, c.lane * 16, base, i * 1024);
  SB();
}

DI void stage_issue(const Ctx &c, int srcbase, int g, v4u (&a)[7]) {
  srcbase = opaque(srcbase + 4 * g * TILE_D);
  const int tt = 8 * c.w + (c.lane & 7), kq = c.lane >> 3;
#pragma unroll
  for (int i = 0; i < 7; ++i) a[i] = LD16C(c.rs, (tt >> 4) * TILE_D + kq * 256 + (tt & 15) * 16, srcbase, i * 2048);
  SB();
}
DI void stage_finish(const Ctx &c, v4u (&a)[7]) {
  const int tt = 8 * c.w + (c.lane & 7), kq = c.lane >> 3;
#pragma unroll
  for (int i = 0; i < 7; ++i)
    *(LAS v4u *)(c.L + L_A + (tt >> 4) * TILE_D + (kq + 8 * i) * 256 + (tt & 15) * 16) = a[i];
  {
    float s1a = 0.f, s1b = 0.f, s2a = 0.f, s2b = 0.f;
#pragma unroll
    for (int i = 0; i < 7; ++i) {
      s1a = dot2(a[i].x, 0x3f803f80u, s1a); s2a = dot2(a[i].x, a[i].x, s2a);
      s1b = dot2(a[i].y, 0x3f803f80u, s1b); s2b = dot2(a[i].y, a[i].y, s2b);
      s1a = dot2(a[i].z, 0x3f803f80u, s1a); s2a = dot2(a[i].z, a[i].z, s2a);
      s1b = dot2(a[i].w, 0x3f803f80u, s1b); s2b = dot2(a[i].w, a[i].w, s2b);
    }
    float s1 = s1a + s1b, s2 = s2a + s2b;
    s1 += dppf<0x128>(s1); s2 += dppf<0x128>(s2);
    s1 = sum_q(s1); s2 = sum_q(s2);
    const LnC lc = ln_coef<false>(s1, s2);
    if (kq == 0) *(LAS f32x2 *)(c.L + L_ST + tt * 8) = f32x2{lc.A, lc.B};
  }
  __syncthreads();
}

template <int MODE>
DI void gemm_dir(const Ctx &c, int src, int tile, int lw, int jm, f32x4 &r0, f32x4 &r1, LnC &lc, v4u &xk) {
  const int lane = c.lane;
  v4u xf[14];
  src = opaque(src + tile * TILE_D);
#pragma unroll
  for (int ks = 0; ks < 14; ++ks) xf[ks] = LD16C(c.rs, lane * 16, src, ks * 1024);
  const LAS unsigned char *wb_ = c.L + lw + lane * 16;
  v4u wf[3][2];
#pragma unroll
  for (int ks = 0; ks < 2; ++ks) { wf[ks][0] = *(const LAS v4u *)(wb_ + ks * 1024); wf[ks][1] = *(const LAS v4u *)(wb_ + (14 + ks) * 1024); }
  SB();
  r0 = f32x4{0.f, 0.f, 0.f, 0.f}; r1 = r0;
  f32x4 ss = r0, gg = r0;
#pragma unroll
  for (int ks = 0; ks < 14; ++ks) {
    if (ks + 2 < 14) {
      wf[(ks + 2) % 3][0] = *(const LAS v4u *)(wb_ + (ks + 2) * 1024);
      wf[(ks + 2) % 3][1] = *(const LAS v4u *)(wb_ + (16 + ks) * 1024);
    }
    SB();
    r0 = mfma32(wf[ks % 3][0], xf[ks], r0);
    r1 = mfma32(wf[ks % 3][1], xf[ks], r1);
    if (MODE == 2) {
      ss = mfma32(v4u{0x3f803f80u, 0x3f803f80u, 0x3f803f80u, 0x3f803f80u}, xf[ks], ss);
      gg = mfma32(xf[ks], xf[ks], gg);
      if (ks == jm) xk = xf[ks];
    }
    SB();
  }
  SB();
  if (MODE == 2) lc = ln_coef<true>(ss[0], gram_diag(gg, lane));
}
DI v4u add_res(v4u x, f32x4 a, f32x4 b) {
  v4u o;
  o.x = pk2(bflo(x.x) + rbf(a[0]), bfhi(x.x) + rbf(a[1])); o.y = pk2(bflo(x.y) + rbf(a[2]), bfhi(x.y) + rbf(a[3]));
  o.z = pk2(bflo(x.z) + rbf(b[0]), bfhi(x.z) + rbf(b[1])); o.w = pk2(bflo(x.w) + rbf(b[2]), bfhi(x.w) + rbf(b[3]));
  return o;
}

DI void dual_softmax(f32x4 p0, f32x4 p1, bool v1, v4u &pos, v4u &neg) {
  float pv[8];
#pragma unroll
  for (int i = 0; i < 4; ++i) { pv[i] = rbf(p0[i]) * LOG2E; pv[4 + i] = rbf(p1[i]) * LOG2E; }
  float mx = fmaxf(fmaxf(pv[0], pv[1]), fmaxf(pv[2], pv[3]));
  float mn = fminf(fminf(pv[0], pv[1]), fminf(pv[2], pv[3]));
  const float mx1 = fmaxf(fmaxf(pv[4], pv[5]), fmaxf(pv[6], pv[7]));
  const float mn1 = fminf(fminf(pv[4], pv[5]), fminf(pv[6], pv[7]));
  if (v1) { mx = fmaxf(mx, mx1); mn = fminf(mn, mn1); }
  mx = max_q(mx); mn = min_q(mn);
  float ep[8], en[8];
#pragma unroll
  for (int i = 0; i < 8; ++i) { ep[i] = ex2(pv[i] - mx); en[i] = ex2(mn - pv[i]); }
  float sp = (ep[0] + ep[1]) + (ep[2] + ep[3]), sn = (en[0] + en[1]) + (en[2] + en[3]);
  const float sp1 = (ep[4] + ep[5]) + (ep[6] + ep[7]), sn1 = (en[4] + en[5]) + (en[6] + en[7]);
  if (v1) { sp += sp1; sn += sn1; }
  sp = sum_q(sp); sn = sum_q(sn);
  const float rp = rcp(sp), rn = rcp(sn);
  pos.x = pk2(ep[0] * rp, ep[1] * rp); pos.y = pk2(ep[2] * rp, ep[3] * rp);
  neg.x = pk2(en[0] * rn, en[1] * rn); neg.y = pk2(en[2] * rn, en[3] * rn);
  pos.z = v1 ? pk2(ep[4] * rp, ep[5] * rp) : 0u; pos.w = v1 ? pk2(ep[6] * rp, ep[7] * rp) : 0u;
  neg.z = v1 ? pk2(en[4] * rn, en[5] * rn) : 0u; neg.w = v1 ? pk2(en[6] * rn, en[7] * rn) : 0u;
}
DI v2u trr(const LAS unsigned char *a) {
  return __builtin_bit_cast(v2u, __builtin_amdgcn_ds_read_tr16_b64_v4i16((LAS bf16x4 *)a));
}

__global__ void __launch_bounds__(NTHR, 2) k_main(Params p) {
  extern __shared__ __attribute__((aligned(16))) unsigned char smem[];
  Ctx c;
  c.rs = __builtin_amdgcn_make_buffer_rsrc((void *)p.ws, 0, WS_END, 0x00020000);
  c.ctrl = (gu32 *)p.ws;
  c.L = (LAS unsigned char *)smem;
  c.tid = threadIdx.x; c.lane = c.tid & 63; c.w = __builtin_amdgcn_readfirstlane(c.tid >> 6);
  const int b = blockIdx.x, xcd = b & 7, r = b >> 3;
  const int g = 4 * xcd + (r >> 3), j = r & 7;
  const int tau = 2 * xcd + (r >> 4), head = r & 15;
  const int w = c.w;
  const int jm = r & 15;
  const int jmc = jm < 14 ? jm : 0;
  const f32x4 z4 = {0.f, 0.f, 0.f, 0.f};

  dma_run<56>(p, c, WQKV_T + head * (84 * 1024) + 28 * 1024, L_WQ + 28 * 1024);
  v4u xf[14];
  {
    const int xb = opaque(XIN + (tau * 8 + w) * TILE_D);
#pragma unroll
    for (int ks = 0; ks < 14; ++ks) xf[ks] = LD16C(c.rs, c.lane * 16, xb, ks * 1024);
  }
  dma_run<28>(p, c, WQKV_T + head * (84 * 1024), L_WQ);
  SB();
  v4u cpf;
  {
    unsigned long long pc;
    asm volatile("s_getpc_b64 %0" : "=s"(pc));
    const int idx = (r * 8 + w) % CODE_PF;
    cpf = *(const GAS v4u *)((pc & ~255ull) + idx * 1024 + c.lane * 16);
  }

  for (int pair = 0; pair < 2; ++pair) {
    const unsigned e1 = pair + 1;
    const int xin = XIN + pair * ACT_D;
    v4u wr[28];
    const int w3b = W3_T + (16 * (j < 7 ? j : 0) + 2 * w) * 14 * 1024;
    f32x4 pq0, pq1;
    {
      FRESH();
      const v4u wph0 = ld16(c.rs, lane * 16, WPHI_T + (head * 2 + 0) * 1024);
      const v4u wph1 = ld16(c.rs, lane * 16, WPHI_T + (head * 2 + 1) * 1024);
      v4u cs[6];
#pragma unroll
      for (int nt = 0; nt < 6; ++nt) cs[nt] = ld16(c.rs, q4 * 16 + nt * 64, CS_QKV + head * 384);
      if (pair > 0) {
        DRAIN();
        wait2(c, C_XN + 2 * tau, C_XN + 2 * tau + 1, 7u * pair);
        const int xb = opaque(xin + (tau * 8 + w) * TILE_D);
#pragma unroll
        for (int ks = 0; ks < 14; ++ks) xf[ks] = LD16C(c.rs, lane * 16, xb, ks * 1024);
      }

      if (pair == 0) {
        DRAIN();
        __syncthreads();
        asm volatile("" :: "v"(cpf));
      }
      const LAS unsigned char *wb_ = c.L + L_WQ + lane * 16;
      f32x4 ss = z4, gg = z4;
      f32x4 acc[4];
#pragma unroll
      for (int nt = 0; nt < 4; ++nt) acc[nt] = z4;
      {
        v4u wf[2][4];
#pragma unroll
        for (int nt = 0; nt < 4; ++nt) wf[0][nt] = *(const LAS v4u *)(wb_ + ((2 + nt) * 14) * 1024);
        SB();
#pragma unroll
        for (int ks = 0; ks < 14; ++ks) {
          if (ks + 1 < 14) {
#pragma unroll
            for (int nt = 0; nt < 4; ++nt) wf[(ks + 1) & 1][nt] = *(const LAS v4u *)(wb_ + ((2 + nt) * 14 + ks + 1) * 1024);
          }
          SB();
#pragma unroll
          for (int nt = 0; nt < 4; ++nt) acc[nt] = mfma32(wf[ks & 1][nt], xf[ks], acc[nt]);
          ss = mfma32(v4u{0x3f803f80u, 0x3f803f80u, 0x3f803f80u, 0x3f803f80u}, xf[ks], ss);
          gg = mfma32(xf[ks], xf[ks], gg);
          SB();
        }
      }

      const LnC lc = ln_coef<false>(ss[0], gram_diag(gg, lane));
      const bool v1 = q4 < 3;
      {
        v2u kv_[4];
#pragma unroll
        for (int nt = 0; nt < 4; ++nt) {
          const f32x4 v = acc[nt] * lc.A - __builtin_bit_cast(f32x4, cs[2 + nt]) * lc.B;
          kv_[nt] = v2u{pk2(v[0], v[1]), pk2(v[2], v[3])};
        }
        const v4u kp = {kv_[0].x, kv_[0].y, kv_[1].x, kv_[1].y};
        v4u pp, pn;
        dual_softmax(mfma32(wph0, kp, z4), mfma32(wph1, kp, z4), v1, pp, pn);
        LAS unsigned char *pk = c.L + L_PK + (16 * w + t16) * PK_STR + q4 * 8;
        *(LAS v2u *)(pk) = v2u{pp.x, pp.y};
        *(LAS v2u *)(pk + 32) = v2u{pp.z, pp.w};
        *(LAS v2u *)(pk + 64) = v2u{pn.x, pn.y};
        *(LAS v2u *)(pk + 96) = v2u{pn.z, pn.w};
        LAS unsigned char *vv = c.L + L_V + (16 * w + t16) * V_STR + q4 * 8;
        *(LAS v2u *)(vv) = kv_[2];
        *(LAS v2u *)(vv + 32) = v1 ? kv_[3] : v2u{0x00003f80u, 0u};
      }
      __syncthreads();

      {
        const int ft = w >> 1, dt = w & 1;
        const int rq = (lane & 15) >> 2, rp = lane & 3;
        const LAS unsigned char *pa = c.L + L_PK + (8 * q4 + rq) * PK_STR + (16 * ft + 4 * rp) * 2;
        const LAS unsigned char *pb = c.L + L_V + (8 * q4 + rq) * V_STR + (16 * dt + 4 * rp) * 2;
        f32x4 kv = z4;
#pragma unroll
        for (int ks = 0; ks < 4; ++ks) {
          const v2u a0 = trr(pa + (32 * ks) * PK_STR), a1 = trr(pa + (32 * ks + 4) * PK_STR);
          const v2u b0 = trr(pb + (32 * ks) * V_STR), b1 = trr(pb + (32 * ks + 4) * V_STR);
          kv = mfma32(v4u{a0.x, a0.y, a1.x, a1.y}, v4u{b0.x, b0.y, b1.x, b1.y}, kv);
        }
        st8(c.rs, lane * 16 + (tau & 1) * 8, KVP_B + ((pair * 16 + head) * 8 + (tau >> 1)) * 8192 + w * 1024, v2u{pk2(kv[0], kv[1]), pk2(kv[2], kv[3])});
      }

      publish(c, C_KV + head);

      dma_run<28>(p, c, WO_T + jmc * (28 * 1024), L_WO);
      {
        f32x4 q0 = z4, q1 = z4;
        v4u wf[3][2];
#pragma unroll
        for (int ks = 0; ks < 2; ++ks) { wf[ks][0] = *(const LAS v4u *)(wb_ + ks * 1024); wf[ks][1] = *(const LAS v4u *)(wb_ + (14 + ks) * 1024); }
        SB();
#pragma unroll
        for (int ks = 0; ks < 14; ++ks) {
          if (ks + 2 < 14) {
            wf[(ks + 2) % 3][0] = *(const LAS v4u *)(wb_ + (ks + 2) * 1024);
            wf[(ks + 2) % 3][1] = *(const LAS v4u *)(wb_ + (16 + ks) * 1024);
          }
          SB();
          q0 = mfma32(wf[ks % 3][0], xf[ks], q0);
          q1 = mfma32(wf[ks % 3][1], xf[ks], q1);
          SB();
        }
        q0 = q0 * lc.A - __builtin_bit_cast(f32x4, cs[0]) * lc.B;
        q1 = q1 * lc.A - __builtin_bit_cast(f32x4, cs[1]) * lc.B;
        const v4u qp = {pk2(q0[0], q0[1]), pk2(q0[2], q0[3]), pk2(q1[0], q1[1]), pk2(q1[2], q1[3])};
        pq0 = mfma32(wph0, qp, z4); pq1 = mfma32(wph1, qp, z4);
      }
#pragma unroll
      for (int ks = 0; ks < 14; ++ks) xf[ks] = v4u{0u, 0u, 0u, 0u};
    }
    {
      FRESH();
      wait1(c, C_KV + head, 16u * e1);

      v4u pr[8];
      const int kb = opaque(KVP_B + (pair * 16 + head) * 8 * 8192 + w * 1024);
#pragma unroll
      for (int t = 0; t < 8; ++t) pr[t] = ld16(c.rs, lane * 16, kb + t * 8192);
      SB();
      v4u phq_p, phq_n;
      dual_softmax(pq0, pq1, q4 < 3, phq_p, phq_n);
      SB();
      f32x4 s = z4;
#pragma unroll
      for (int t = 0; t < 8; ++t) {
        s += f32x4{bflo(pr[t].x), bfhi(pr[t].x), bflo(pr[t].y), bfhi(pr[t].y)};
        s += f32x4{bflo(pr[t].z), bfhi(pr[t].z), bflo(pr[t].w), bfhi(pr[t].w)};
      }
      {
        const int ft = w >> 1, dt = w & 1, d = 16 * dt + t16, part = ft >> 1, half = ft & 1;
        *(LAS v2u *)(c.L + L_KVS + ((d * 2 + part) * 4 + q4) * 16 + half * 8) = v2u{pk2(s[0], s[1]), pk2(s[2], s[3])};
      }
      __syncthreads();
      f32x4 nm[2];
#pragma unroll
      for (int dt = 0; dt < 2; ++dt) {
        const v4u ap = *(const LAS v4u *)(c.L + L_KVS + (((16 * dt + t16) * 2 + 0) * 4 + q4) * 16);
        const v4u an = *(const LAS v4u *)(c.L + L_KVS + (((16 * dt + t16) * 2 + 1) * 4 + q4) * 16);
        nm[dt] = mfma32(ap, phq_p, z4);
        nm[dt] = mfma32(an, phq_n, nm[dt]);
      }
      const float den0 = __shfl(nm[1][0], 48 + t16);
      const float rden = rcp(rbf(rbf(den0) + 1e-6f));
#pragma unroll
      for (int dt = 0; dt < 2; ++dt) {
        if (dt == 0 || q4 < 3) {
          const int col = DH * head + 16 * dt + 4 * q4;
          v2u o;
          o.x = pk2(rbf(nm[dt][0]) * rden, rbf(nm[dt][1]) * rden);
          o.y = pk2(rbf(nm[dt][2]) * rden, rbf(nm[dt][3]) * rden);
          st8(c.rs, (col >> 3) * 256 + t16 * 16 + ((col >> 2) & 1) * 8, O_B + (tau * 8 + w) * TILE_D, o);
        }
      }

      publish(c, C_O + tau);

      dma_run<28>(p, c, W1_T + jmc * (28 * 1024), L_W1);
      dma_run<28>(p, c, W2_T + jmc * (28 * 1024), L_W2);
    }
    if (jm < 14) {
      const int tile = tau * 8 + w;
      v4u yk;
      {
        FRESH();
        const int eo = tile * TILE_D + jm * 1024;
        const v4u xr = ld16(c.rs, lane * 16, xin + eo);
        wait1(c, C_O + tau, 16u * e1);
        f32x4 r0, r1; LnC lc; v4u xk;
        gemm_dir<0>(c, O_B, tile, L_WO, jm, r0, r1, lc, xk);

        st16(c.rs, lane * 16, XP_B + eo, add_res(xr, r0, r1));
        publish(c, C_XP + tau);
        load_run<0, 10, 28>(c, w3b, wr);
      }
      {
        FRESH();
        const int eo = tile * TILE_D + jm * 1024;
        const f32x4 cs0 = __builtin_bit_cast(f32x4, ld16(c.rs, q4 * 32, CS_W1 + jm * 128));
        const f32x4 cs1 = __builtin_bit_cast(f32x4, ld16(c.rs, q4 * 32 + 16, CS_W1 + jm * 128));
        wait1(c, C_XP + tau, 14u * e1);
        f32x4 r0, r1; LnC lc; v4u xk;
        gemm_dir<2>(c, XP_B, tile, L_W1, jm, r0, r1, lc, xk);

        r0 = r0 * lc.A - cs0 * lc.B;
        r1 = r1 * lc.A - cs1 * lc.B;
        {
          const int k8 = 4 * jm + q4, cg = k8 / 14;
          const int d = (cg == 0) ? 1 : (cg == 1) ? -1 : (cg == 2) ? 2 : -2;
          const int tok = 16 * tile + t16, sl = tok + d;
          const int ho = (sl < 0 || sl >= S_) ? 0x7ff00000 : (sl >> 4) * TILE_D + k8 * 256 + (sl & 15) * 16;
          st16(c.rs, ho, H_B, v4u{gelu2(r0[0], r0[1]), gelu2(r0[2], r0[3]), gelu2(r1[0], r1[1]), gelu2(r1[2], r1[3])});
          const int src = tok - d;
          if (src < 0 || src >= S_) st16(c.rs, (tok >> 4) * TILE_D + k8 * 256 + (tok & 15) * 16, H_B, v4u{0u, 0u, 0u, 0u});
        }
#pragma unroll
        for (int e = 0; e < 4; ++e) {
          const unsigned d = pk2(bflo(xk[e]) - lc.mb, bfhi(xk[e]) - lc.mb);
          yk[e] = pk2(bflo(d) * lc.r, bfhi(d) * lc.r);
        }
        publish(c, C_H + tau);
        load_run<10, 19, 28>(c, w3b, wr);
      }
      {
        FRESH();
        const int eo = tile * TILE_D + jm * 1024;
        wait3(c, C_H + tau, C_H + (tau > 0 ? tau - 1 : tau), C_H + (tau < 15 ? tau + 1 : tau), 14u * e1);
        f32x4 r0, r1; LnC lc; v4u xk;
        gemm_dir<0>(c, H_B, tile, L_W2, jm, r0, r1, lc, xk);

        st16(c.rs, lane * 16, X1_B + eo, add_res(yk, r0, r1));
        publish(c, C_X1 + tau);
        load_run<19, 28, 28>(c, w3b, wr);
      }
    } else {
      FRESH();
      load_run<0, 28, 28>(c, w3b, wr);
    }
    if (j < 7) {
      {
        FRESH();
        const f32x4 cs0 = __builtin_bit_cast(f32x4, ld16(c.rs, q4 * 32, CS_W3 + (256 * j + 32 * w) * 4));
        const f32x4 cs1 = __builtin_bit_cast(f32x4, ld16(c.rs, q4 * 32 + 16, CS_W3 + (256 * j + 32 * w) * 4));
        wait1(c, C_X1 + (g >> 1), 14u * e1);
        {
          v4u a[7];
          stage_issue(c, X1_B, g, a);
          stage_finish(c, a);
        }
        f32x4 acc[4][2];
#pragma unroll
        for (int m = 0; m < 4; ++m) { acc[m][0] = z4; acc[m][1] = z4; }
        const int w4b = opaque(W4_T + (4 * j * 56 + 7 * w) * 1024);
        {
          v4u bf[2][4];
          const LAS unsigned char *ab = c.L + L_A + lane * 16;
#pragma unroll
          for (int m = 0; m < 4; ++m) bf[0][m] = *(const LAS v4u *)(ab + m * TILE_D);
#pragma unroll
          for (int ks = 0; ks < 14; ++ks) {
            if (ks + 1 < 14) {
#pragma unroll
              for (int m = 0; m < 4; ++m) bf[(ks + 1) & 1][m] = *(const LAS v4u *)(ab + m * TILE_D + (ks + 1) * 1024);
            }
            SB();
#pragma unroll
            for (int m = 0; m < 4; ++m) {
              acc[m][0] = mfma32(wr[ks], bf[ks & 1][m], acc[m][0]);
              acc[m][1] = mfma32(wr[14 + ks], bf[ks & 1][m], acc[m][1]);
            }
            SB();
            wr[ks] = LD16C(c.rs, lane * 16, w4b, ((ks / 7) * 56 + ks % 7) * 1024);
            wr[14 + ks] = LD16C(c.rs, lane * 16, w4b, ((2 + ks / 7) * 56 + ks % 7) * 1024);
          }
          SB();
        }
#pragma unroll
        for (int m = 0; m < 4; ++m) {
          const f32x2 st = *(const LAS f32x2 *)(c.L + L_ST + (16 * m + t16) * 8);
          const f32x4 u0 = acc[m][0] * st.x - cs0 * st.y, u1 = acc[m][1] * st.x - cs1 * st.y;
          const v4u o = {gelu2(u0[0], u0[1]), gelu2(u0[2], u0[3]), gelu2(u1[0], u1[1]), gelu2(u1[2], u1[3])};
          st16(c.rs, lane * 16, G_B + (4 * g + m) * TILE_F + (32 * j + 4 * w) * 256, o);
        }
        publish(c, C_G + g);
      }
      {
        FRESH();
        const int roff = q4 * TILE_D + t16 * 16;
        const int rso = 4 * g * TILE_D + (8 * j + w) * 256;
        const v4u rxp = ld16(c.rs, roff, XP_B + rso);
        const v4u rx1 = ld16(c.rs, roff, X1_B + rso);
        wait1(c, C_G + g, 7u * e1);
        const int abase = opaque(G_B + 4 * g * TILE_F + 7 * w * 1024);
        constexpr int RING = 14;
        v4u a[RING];
#pragma unroll
        for (int i = 0; i < RING; ++i) a[i] = LD16C(c.rs, lane * 16, abase, (i / 7) * TILE_F + (i % 7) * 1024);
        SB();
#pragma unroll
        for (int m = 0; m < 4; ++m) {
          f32x4 acc[4];
#pragma unroll
          for (int t = 0; t < 4; ++t) acc[t] = z4;
#pragma unroll
          for (int kk = 0; kk < 7; ++kk) {
            const int i = m * 7 + kk;
            const v4u cur = a[i % RING];
            if (i + RING < 28) a[i % RING] = LD16C(c.rs, lane * 16, abase, ((i + RING) / 7) * TILE_F + ((i + RING) % 7) * 1024);
#pragma unroll
            for (int t = 0; t < 4; ++t) acc[t] = mfma32(wr[t * 7 + kk], cur, acc[t]);
            SB();
          }
#pragma unroll
          for (int t = 0; t < 4; ++t) *(LAS f32x4 *)(c.L + L_RED + ((w * 16 + m * 4 + t) * 64 + lane) * 16) = acc[t];
        }
        if (pair == 0) {
          const int qb = opaque(WQKV_T + head * (84 * 1024) + w * 1024);
#pragma unroll
          for (int i = 0; i < 11; ++i) wr[i] = ld16(c.rs, lane * 16, qb + (w + 8 * i < 84 ? i * 8192 : 0));
          SB();
        }

        __syncthreads();
        f32x4 s0 = z4, s1 = z4;
        {
          const int t = w >> 1, qq = 2 * (w & 1);
#pragma unroll
          for (int ww = 0; ww < 8; ++ww) {
            s0 += *(const LAS f32x4 *)(c.L + L_RED + ((ww * 16 + q4 * 4 + t) * 64 + t16 + 16 * qq) * 16);
            s1 += *(const LAS f32x4 *)(c.L + L_RED + ((ww * 16 + q4 * 4 + t) * 64 + t16 + 16 * (qq + 1)) * 16);
          }
        }
        v4u o;
        o.x = pk2(bflo(rxp.x) + rbf(bflo(rx1.x) + rbf(s0[0])), bfhi(rxp.x) + rbf(bfhi(rx1.x) + rbf(s0[1])));
        o.y = pk2(bflo(rxp.y) + rbf(bflo(rx1.y) + rbf(s0[2])), bfhi(rxp.y) + rbf(bfhi(rx1.y) + rbf(s0[3])));
        o.z = pk2(bflo(rxp.z) + rbf(bflo(rx1.z) + rbf(s1[0])), bfhi(rxp.z) + rbf(bfhi(rx1.z) + rbf(s1[1])));
        o.w = pk2(bflo(rxp.w) + rbf(bflo(rx1.w) + rbf(s1[2])), bfhi(rxp.w) + rbf(bfhi(rx1.w) + rbf(s1[3])));

        if (pair == 0) {
          st16(c.rs, roff, XIN + ACT_D + rso, o);
          publish(c, C_XN + g);
#pragma unroll
          for (int i = 0; i < 11; ++i)
            if (w + 8 * i < 84) *(LAS v4u *)(c.L + L_WQ + (w + 8 * i) * 1024 + lane * 16) = wr[i];
        } else {
          *(v4u *)(p.out + (size_t)(64 * g + 16 * q4 + t16) * D_ + 64 * j + 8 * w) = o;
        }

      }
    }
    else if (pair == 0) {
      DRAIN();
      __syncthreads();
      c.lane = opaque_v(c.lane);
      dma_run<84>(p, c, WQKV_T + head * (84 * 1024), L_WQ);
    }
  }
}

}

extern "C" void kernel_launch(void *const *d_in, const int *in_sizes, int n_in, void *d_out, int out_size, void *d_ws,
                              size_t ws_size, hipStream_t stream) {
  Params p;
  p.x = (const unsigned short *)d_in[0];
  p.wqkv = (const unsigned short *)d_in[1];
  p.wphi = (const unsigned short *)d_in[2];
  p.wo = (const unsigned short *)d_in[3];
  p.w1 = (const unsigned short *)d_in[5];
  p.w2 = (const unsigned short *)d_in[6];
  p.w3 = (const unsigned short *)d_in[7];
  p.w4 = (const unsigned short *)d_in[8];
  p.out = (unsigned short *)d_out;
  p.ws = (unsigned char *)d_ws;
  (void)hipFuncSetAttribute((const void *)k_main, hipFuncAttributeMaxDynamicSharedMemorySize, LDS_BYTES);
  hipLaunchKernelGGL(k_prep, dim3(PREP_BLOCKS), dim3(256), 0, stream, p);
  hipLaunchKernelGGL(k_main, dim3(256), dim3(NTHR), LDS_BYTES, stream, p);
}
```

```cpp
#include <hip/hip_runtime.h>
#include <hip/hip_bf16.h>
#include <stdint.h>

#define GAS __attribute__((address_space(1)))
#define LAS __attribute__((address_space(3)))
#define RLX_AGENT __ATOMIC_RELAXED, __HIP_MEMORY_SCOPE_AGENT
#define DI __device__ __forceinline__
typedef unsigned v4u __attribute__((ext_vector_type(4)));
typedef unsigned v2u __attribute__((ext_vector_type(2)));
typedef float f32x4 __attribute__((ext_vector_type(4)));
typedef float f32x2 __attribute__((ext_vector_type(2)));
typedef short bf16x8 __attribute__((ext_vector_type(8)));
typedef short bf16x4 __attribute__((ext_vector_type(4)));
typedef __bf16 bfv2 __attribute__((ext_vector_type(2)));
typedef GAS unsigned gu32;

namespace {

constexpr int S_ = 2048, D_ = 448, DH = 28, F_ = 1792;
constexpr int NTHR = 512;
constexpr int TILE_D = (D_ / 8) * 256;
constexpr int TILE_F = (F_ / 8) * 256;
constexpr int ACT_D = 128 * TILE_D;
constexpr int ACT_F = 128 * TILE_F;

constexpr int CSTR = 64;
constexpr int C_XN = 1;
constexpr int C_KV = C_XN + 32;
constexpr int C_O = C_KV + 16;
constexpr int C_XP = C_O + 16;
constexpr int C_H = C_XP + 16;
constexpr int C_X1 = C_H + 16;
constexpr int C_G = C_X1 + 16;
constexpr int C_NCTR = C_G + 32;
constexpr int CTRL_BYTES = ((C_NCTR * CSTR * 4 + 65535) / 65536) * 65536;
static_assert(C_NCTR * CSTR * 4 <= CTRL_BYTES, "control block");
constexpr int WQKV_T = CTRL_BYTES;
constexpr int WO_T = WQKV_T + 96 * 14 * 1024;
constexpr int W1_T = WO_T + 28 * 14 * 1024;
constexpr int W2_T = W1_T + 28 * 14 * 1024;
constexpr int W3_T = W2_T + 28 * 14 * 1024;
constexpr int W4_T = W3_T + 112 * 14 * 1024;
constexpr int WPHI_T = W4_T + 28 * 56 * 1024;
constexpr int CS_QKV = WPHI_T + 16 * 2 * 1024;
constexpr int CS_W1 = CS_QKV + 96 * 16 * 4;
constexpr int CS_W3 = CS_W1 + 448 * 4;
constexpr int W_END = CS_W3 + 1792 * 4;
constexpr int XIN = (W_END + 4095) & ~4095;
constexpr int O_B = XIN + 2 * ACT_D;
constexpr int XP_B = O_B + 2 * ACT_D;
constexpr int H_B = XP_B + 2 * ACT_D;
constexpr int X1_B = H_B + 2 * ACT_D;
constexpr int G_B = X1_B + 2 * ACT_D;
constexpr int KVP_B = G_B + 2 * ACT_F;
constexpr int WS_END = KVP_B + 2 * 16 * 16 * 8192;

constexpr int L_A = 0;
constexpr int L_ST = 57344;
constexpr int L_RED = 0;
constexpr int L_WQ = 65536;
constexpr int PK_STR = 136, V_STR = 72;
constexpr int L_PK = 0;
constexpr int L_V = 17408;
constexpr int L_KVS = 26624;
constexpr int L_WO = 32768;
constexpr int L_W1 = 65536;
constexpr int L_W2 = 94208;
constexpr int LDS_BYTES = 151552;

constexpr int CODE_PF = 18;
constexpr unsigned kSpinCap = 1u << 22;

struct Params {
  const unsigned short *x, *wqkv, *wphi, *wo, *w1, *w2, *w3, *w4;
  unsigned short *out;
  unsigned char *ws;
};

DI float asf(unsigned u) { return __builtin_bit_cast(float, u); }
DI unsigned asu(float f) { return __builtin_bit_cast(unsigned, f); }
DI float bflo(unsigned u) { return asf(u << 16); }
DI float bfhi(unsigned u) { return asf(u & 0xffff0000u); }
DI unsigned pk2(float a, float b) {
  f32x2 v = {a, b};
  return __builtin_bit_cast(unsigned, __builtin_convertvector(v, bfv2));
}
DI float rbf(float a) { return (float)(__bf16)a; }
DI float rcp(float a) { return __builtin_amdgcn_rcpf(a); }
DI float ex2(float a) { return __builtin_amdgcn_exp2f(a); }
DI float dot2(unsigned a, unsigned b, float c) {
  return __builtin_amdgcn_fdot2_f32_bf16(__builtin_bit_cast(bfv2, a), __builtin_bit_cast(bfv2, b), c, false);
}
DI f32x4 mfma32(v4u a, v4u b, f32x4 c) {
  return __builtin_amdgcn_mfma_f32_16x16x32_bf16(__builtin_bit_cast(bf16x8, a), __builtin_bit_cast(bf16x8, b), c, 0, 0, 0);
}
#define DRAIN() asm volatile("s_waitcnt vmcnt(0)" ::: "memory")
constexpr int SC1 = 16;
DI v4u ld16(__amdgpu_buffer_rsrc_t rs, int voff, int soff) { return __builtin_amdgcn_raw_buffer_load_b128(rs, voff, soff, SC1); }
DI v2u ld8(__amdgpu_buffer_rsrc_t rs, int voff, int soff) { return __builtin_amdgcn_raw_buffer_load_b64(rs, voff, soff, SC1); }
DI void st16(__amdgpu_buffer_rsrc_t rs, int voff, int soff, v4u v) { __builtin_amdgcn_raw_buffer_store_b128(v, rs, voff + soff, 0, SC1); }
DI void st8(__amdgpu_buffer_rsrc_t rs, int voff, int soff, v2u v) { __builtin_amdgcn_raw_buffer_store_b64(v, rs, voff, soff, SC1); }
DI void st4(__amdgpu_buffer_rsrc_t rs, int voff, int soff, unsigned v) { __builtin_amdgcn_raw_buffer_store_b32(v, rs, voff, soff, SC1); }
#define LD16C(rs, voff, soff, co) ld16(rs, (voff) + ((co) & 4095), (soff) + ((co) & ~4095))
DI int opaque(int x) { asm volatile("" : "+s"(x)); return x; }
DI int opaque_v(int x) { asm volatile("" : "+v"(x)); return x; }
#define FRESH() c.lane = opaque_v(c.lane); const int lane = c.lane, t16 = lane & 15, q4 = lane >> 4; (void)t16; (void)q4;
#define SB() __builtin_amdgcn_sched_barrier(0)

template <int CTRL> DI float dppf(float v) {
  return asf(__builtin_amdgcn_update_dpp(0, (int)asu(v), CTRL, 0xf, 0xf, false));
}
DI float sum_q(float v) {
  auto r = __builtin_amdgcn_permlane16_swap(asu(v), asu(v), false, false);
  v = asf(r[0]) + asf(r[1]);
  auto s = __builtin_amdgcn_permlane32_swap(asu(v), asu(v), false, false);
  return asf(s[0]) + asf(s[1]);
}
DI float max_q(float v) {
  auto r = __builtin_amdgcn_permlane16_swap(asu(v), asu(v), false, false);
  v = fmaxf(asf(r[0]), asf(r[1]));
  auto s = __builtin_amdgcn_permlane32_swap(asu(v), asu(v), false, false);
  return fmaxf(asf(s[0]), asf(s[1]));
}
DI float min_q(float v) {
  auto r = __builtin_amdgcn_permlane16_swap(asu(v), asu(v), false, false);
  v = fminf(asf(r[0]), asf(r[1]));
  auto s = __builtin_amdgcn_permlane32_swap(asu(v), asu(v), false, false);
  return fminf(asf(s[0]), asf(s[1]));
}

constexpr float LOG2E = 1.4426950408889634f;
DI unsigned gelu2(float a, float b) {
  const f32x2 x = {a, b};
  constexpr float c0 = -2.0f * 0.7978845608028654f * LOG2E, c1 = c0 * 0.044715f;
  const f32x2 u = x * (x * x * c1 + c0);
  f32x2 e = {ex2(u.x), ex2(u.y)};
  e = e + 1.0f;
  const f32x2 r = {rcp(e.x), rcp(e.y)};
  const f32x2 o = x * r;
  return pk2(o.x, o.y);
}

DI void poll_ge(gu32 *w, unsigned want, gu32 *tmo) {
#pragma nounroll
  for (unsigned spins = 0;;) {
    if (__hip_atomic_load(w, RLX_AGENT) >= want) return;
    __builtin_amdgcn_s_sleep(1);
    if (++spins > kSpinCap) { __hip_atomic_store(tmo, 1u, RLX_AGENT); return; }
    if ((spins & 1023u) == 0u && __hip_atomic_load(tmo, RLX_AGENT) != 0u) return;
  }
}

struct LnC { float A, B, mb, r; };
template <bool TWICE> DI LnC ln_coef(float s1, float s2) {
  const float mean = s1 * (1.0f / D_);
  const float var = fmaxf(s2 * (1.0f / D_) - mean * mean, 0.f);
  LnC o;
  o.mb = rbf(mean);
  const float sd = rbf(__builtin_amdgcn_sqrtf(rbf(rbf(var) + 1e-5f)));
  o.r = rcp(sd);
  if (TWICE) {
    const float my = (mean - o.mb) * o.r, vy = var * o.r * o.r;
    const float mby = rbf(my);
    const float ry = rcp(rbf(__builtin_amdgcn_sqrtf(rbf(rbf(vy) + 1e-5f))));
    o.A = o.r * ry;
    o.B = (o.mb * o.r + mby) * ry;
  } else {
    o.A = o.r;
    o.B = o.mb * o.r;
  }
  return o;
}

DI float gram_diag(f32x4 gg, int lane) {
  const int t16 = lane & 15, q4 = lane >> 4;
  const float a = (t16 & 1) ? gg[1] : gg[0], b = (t16 & 1) ? gg[3] : gg[2];
  const float d = (t16 & 2) ? b : a;
  return sum_q(q4 == (t16 >> 2) ? d : 0.f);
}

constexpr int NB_X = S_ * (D_ / 8) / 256;
constexpr int NJ_Q = 3 * 4 * 14;
constexpr int NJ_S = 3 * 7 * 14, NJ_3 = 28 * 14, NJ_4 = 7 * 56;
constexpr int NB_W = NJ_Q + NJ_S + NJ_3 + NJ_4;
constexpr int NB_PHI = 8;
constexpr int NB_CS = 21 + 7 + 28;
#define PJ 2
constexpr int PREP_BLOCKS = (NB_W + NB_CS + NB_PHI + NB_X) / PJ;
static_assert(NJ_Q % PJ == 0 && NB_W % PJ == 0 && NB_CS % PJ == 0 && NB_PHI % PJ == 0 && NB_X % PJ == 0, "both halves of a block take the same path");

__global__ void __launch_bounds__(256 * PJ) k_prep(Params p) {
  __shared__ __attribute__((aligned(16))) unsigned char sm_[PJ * 32 * 240];
  unsigned char *sm = sm_ + (threadIdx.x >> 8) * (32 * 240);
  const int vb = blockIdx.x * PJ + (threadIdx.x >> 8);
  f32x4 *red = (f32x4 *)sm;
  const __amdgpu_buffer_rsrc_t rs = __builtin_amdgcn_make_buffer_rsrc((void *)p.ws, 0, WS_END, 0x00020000);
  const int tid = threadIdx.x & 255;
  int b = vb < NB_CS + NB_PHI ? NB_W + vb : (vb < NB_CS + NB_PHI + NB_W ? vb - (NB_CS + NB_PHI) : vb);
  if (b < NJ_Q) {
    const int ks = b % 14, hg = (b / 14) & 3, part = b / 56;
    const unsigned short *src = p.wqkv + (size_t)(ks * 32) * (3 * D_) + part * D_ + hg * 112;
#pragma unroll
    for (int i = 0; i < 2; ++i) {
      const int ch = tid + 256 * i;
      if (ch < 448) {
        const int row = ch / 14, cc = ch % 14;
        *(v4u *)(sm + row * 240 + cc * 16) = *(const v4u *)(src + (size_t)row * (3 * D_) + cc * 8);
      }
    }
    __syncthreads();
#pragma unroll
    for (int i = 0; i < 2; ++i) {
      const int f = (tid >> 6) + 4 * i, slot = tid & 63, r = slot & 15, kg = slot >> 4;
      const int cl = 16 * (f & 1) + r;
      const bool valid = cl < DH;
      const unsigned short *q_ = (const unsigned short *)(sm + (8 * kg) * 240) + 28 * (f >> 1) + (valid ? cl : 0);
      v4u o;
#pragma unroll
      for (int jj = 0; jj < 4; ++jj) o[jj] = valid ? (unsigned)q_[(2 * jj) * 120] | ((unsigned)q_[(2 * jj + 1) * 120] << 16) : 0u;
      const int nt = (4 * hg + (f >> 1)) * 6 + part * 2 + (f & 1);
      st16(rs, WQKV_T + (nt * 14 + ks) * 1024 + slot * 16, 0, o);
    }
    return;
  }
  if (b < NB_W) {
    int jb = b - NJ_Q;
    const unsigned short *src; int ld, dst, cb, ks, KS; bool perm = true;
    if (jb < NJ_S) {
      const int m = jb / 98, rr = jb % 98;
      cb = rr / 14; ks = rr % 14; KS = 14;
      src = m == 0 ? p.wo : (m == 1 ? p.w1 : p.w2); ld = D_; dst = WO_T + m * (28 * 14 * 1024);
    } else if (jb < NJ_S + NJ_3) {
      jb -= NJ_S; cb = jb / 14; ks = jb % 14; KS = 14; src = p.w3; ld = F_; dst = W3_T;
    } else {
      jb -= NJ_S + NJ_3; cb = jb / 56; ks = jb % 56; KS = 56; src = p.w4; ld = D_; dst = W4_T; perm = false;
    }
    {
      const int row = tid >> 3, cc = tid & 7;
      *(v4u *)(sm + row * 144 + cc * 16) = *(const v4u *)(src + (size_t)(ks * 32 + row) * ld + cb * 64 + cc * 8);
    }
    __syncthreads();
    const int f = tid >> 6, slot = tid & 63, r = slot & 15, kg = slot >> 4;
    const int cl = perm ? 32 * (f >> 1) + 8 * (r >> 2) + 4 * (f & 1) + (r & 3) : 16 * f + r;
    const unsigned short *q_ = (const unsigned short *)(sm + (8 * kg) * 144) + cl;
    v4u o;
#pragma unroll
    for (int jj = 0; jj < 4; ++jj) o[jj] = (unsigned)q_[(2 * jj) * 72] | ((unsigned)q_[(2 * jj + 1) * 72] << 16);
    st16(rs, dst + ((4 * cb + f) * KS + ks) * 1024 + slot * 16, 0, o);
    return;
  }
  b -= NB_W;
  if (b < NB_CS) {
    const unsigned short *src; int ld, col0;
    if (b < 21) { src = p.wqkv; ld = 3 * D_; col0 = b * 64; }
    else if (b < 28) { src = p.w1; ld = D_; col0 = (b - 21) * 64; }
    else { src = p.w3; ld = F_; col0 = (b - 28) * 64; }
    const int n4 = tid & 15, ksl = tid >> 4;
    const unsigned short *s0 = src + (size_t)(ksl * 28) * ld + col0 + 4 * n4;
    f32x4 s = {0.f, 0.f, 0.f, 0.f};
    v2u e[28];
#pragma unroll
    for (int k = 0; k < 28; ++k) e[k] = *(const v2u *)(s0 + (size_t)k * ld);
    SB();
#pragma unroll
    for (int k = 0; k < 28; ++k) s += f32x4{bflo(e[k].x), bfhi(e[k].x), bflo(e[k].y), bfhi(e[k].y)};
    red[tid] = s;
    __syncthreads();
    if (tid < 64) {
      float a = 0.f;
#pragma unroll
      for (int k = 0; k < 16; ++k) a += ((const float *)red)[(k * 16 + (tid >> 2)) * 4 + (tid & 3)];
      const int col = col0 + tid;
      int off;
      if (b < 21) {
        const int part = col / D_, rem = col % D_, head = rem / DH, cc = rem % DH;
        off = CS_QKV + ((head * 6 + part * 2 + (cc >> 4)) * 16 + (cc & 15)) * 4;
      } else if (b < 28) off = CS_W1 + col * 4;
      else off = CS_W3 + col * 4;
      st4(rs, off, 0, asu(a));
    } else if (b == 0 && tid >= 64) {
      const int i = tid - 64;
      if (i < 192) st4(rs, CS_QKV + (((i >> 2) * 2 + 1) * 16 + 12 + (i & 3)) * 4, 0, 0u);
    }
    return;
  }
  b -= NB_CS;
  if (b < NB_PHI) {
    const int pid = b * 256 + tid, fr = pid >> 6, lane = pid & 63, r = lane & 15, kg = lane >> 4;
    const int h = fr >> 1, f = (fr & 1) * 16 + r;
    unsigned short e[8];
#pragma unroll
    for (int j = 0; j < 8; ++j) {
      const int d = (j < 4) ? 4 * kg + j : 16 + 4 * kg + (j - 4);
      e[j] = (d < DH && f < DH) ? p.wphi[(h * DH + d) * DH + f] : (unsigned short)0;
    }
    v4u v;
    v.x = e[0] | ((unsigned)e[1] << 16); v.y = e[2] | ((unsigned)e[3] << 16);
    v.z = e[4] | ((unsigned)e[5] << 16); v.w = e[6] | ((unsigned)e[7] << 16);
    st16(rs, WPHI_T + pid * 16, 0, v);
    if (pid < C_NCTR) __hip_atomic_store((gu32 *)p.ws + CSTR * pid, 0u, RLX_AGENT);
    return;
  }
  b -= NB_PHI;
  {
    const int fr = b * 4 + (tid >> 6), tile = fr / 14, ks = fr % 14, lane = tid & 63;
    const v4u v = *(const v4u *)(p.x + (size_t)(tile * 16 + (lane & 15)) * D_ + ks * 32 + (lane >> 4) * 8);
    st16(rs, XIN + tile * TILE_D + ks * 1024 + lane * 16, 0, v);
  }
}

struct Ctx {
  __amdgpu_buffer_rsrc_t rs;
  gu32 *ctrl;
  LAS unsigned char *L;
  int tid, lane, w;
};

DI void wait1(const Ctx &c, int ctr, unsigned want) {
  if (c.tid == NTHR - 64) {
    poll_ge(c.ctrl + CSTR * ctr, want, c.ctrl);
    __builtin_amdgcn_fence(__ATOMIC_ACQUIRE, "wavefront");
  }
  __syncthreads();
}
DI void wait2(const Ctx &c, int c0, int c1, unsigned want) {
  if (c.tid >= NTHR - 64 && c.tid < NTHR - 62) {
    poll_ge(c.ctrl + CSTR * (c.tid == NTHR - 64 ? c0 : c1), want, c.ctrl);
    __builtin_amdgcn_fence(__ATOMIC_ACQUIRE, "wavefront");
  }
  __syncthreads();
}
DI void wait3(const Ctx &c, int c0, int c1, int c2, unsigned want) {
  if (c.tid >= NTHR - 64 && c.tid < NTHR - 61) {
    poll_ge(c.ctrl + CSTR * (c.tid == NTHR - 64 ? c0 : c.tid == NTHR - 63 ? c1 : c2), want, c.ctrl);
    __builtin_amdgcn_fence(__ATOMIC_ACQUIRE, "wavefront");
  }
  __syncthreads();
}
DI void publish(const Ctx &c, int ctr) {
  DRAIN();
  __syncthreads();
  if (c.tid == 0) __hip_atomic_fetch_add(c.ctrl + CSTR * ctr, 1u, RLX_AGENT);
}

DI void dma1k(const unsigned char *gsrc, unsigned lds_dst) {
  unsigned keep;
  asm volatile("s_mov_b32 %0, m0\n\ts_mov_b32 m0, %2\n\ts_nop 0\n\tglobal_load_lds_dwordx4 %1, off sc1\n\ts_mov_b32 m0, %0"
               : "=&s"(keep) : "v"(gsrc), "s"(lds_dst) : "memory");
}
template <int NFR> DI void dma_run(const Params &p, const Ctx &c, int src, int dst) {
  const unsigned char *g = p.ws + src + c.w * 1024 + c.lane * 16;
  const unsigned l = (unsigned)(size_t)c.L + dst + c.w * 1024;
#pragma unroll
  for (int i = 0; i < (NFR + 7) / 8; ++i) {
    if (c.w + 8 * i < NFR) dma1k(g + i * 8192, l + i * 8192);
  }
}

template <int LO, int HI, int N> DI void load_run(const Ctx &c, int base, v4u (&w)[N]) {
  base = opaque(base);
#pragma unroll
  for (int i = LO; i < HI; ++i) w[i] = LD16C(c.rs, c.lane * 16, base, i * 1024);
  SB();
}

DI void stage_issue(const Ctx &c, int srcbase, int g, v4u (&a)[7]) {
  srcbase = opaque(srcbase + 4 * g * TILE_D);
  const int tt = 8 * c.w + (c.lane & 7), kq = c.lane >> 3;
#pragma unroll
  for (int i = 0; i < 7; ++i) a[i] = LD16C(c.rs, (tt >> 4) * TILE_D + kq * 256 + (tt & 15) * 16, srcbase, i * 2048);
  SB();
}
DI void stage_finish(const Ctx &c, v4u (&a)[7]) {
  const int tt = 8 * c.w + (c.lane & 7), kq = c.lane >> 3;
#pragma unroll
  for (int i = 0; i < 7; ++i)
    *(LAS v4u *)(c.L + L_A + (tt >> 4) * TILE_D + (kq + 8 * i) * 256 + (tt & 15) * 16) = a[i];
  {
    float s1a = 0.f, s1b = 0.f, s2a = 0.f, s2b = 0.f;
#pragma unroll
    for (int i = 0; i < 7; ++i) {
      s1a = dot2(a[i].x, 0x3f803f80u, s1a); s2a = dot2(a[i].x, a[i].x, s2a);
      s1b = dot2(a[i].y, 0x3f803f80u, s1b); s2b = dot2(a[i].y, a[i].y, s2b);
      s1a = dot2(a[i].z, 0x3f803f80u, s1a); s2a = dot2(a[i].z, a[i].z, s2a);
      s1b = dot2(a[i].w, 0x3f803f80u, s1b); s2b = dot2(a[i].w, a[i].w, s2b);
    }
    float s1 = s1a + s1b, s2 = s2a + s2b;
    s1 += dppf<0x128>(s1); s2 += dppf<0x128>(s2);
    s1 = sum_q(s1); s2 = sum_q(s2);
    const LnC lc = ln_coef<false>(s1, s2);
    if (kq == 0) *(LAS f32x2 *)(c.L + L_ST + tt * 8) = f32x2{lc.A, lc.B};
  }
  __syncthreads();
}

template <int MODE>
DI void gemm_dir(const Ctx &c, int src, int tile, int lw, int jm, f32x4 &r0, f32x4 &r1, LnC &lc, v4u &xk) {
  const int lane = c.lane;
  v4u xf[14];
  src = opaque(src + tile * TILE_D);
#pragma unroll
  for (int ks = 0; ks < 14; ++ks) xf[ks] = LD16C(c.rs, lane * 16, src, ks * 1024);
  const LAS unsigned char *wb_ = c.L + lw + lane * 16;
  v4u wf[3][2];
#pragma unroll
  for (int ks = 0; ks < 2; ++ks) { wf[ks][0] = *(const LAS v4u *)(wb_ + ks * 1024); wf[ks][1] = *(const LAS v4u *)(wb_ + (14 + ks) * 1024); }
  SB();
  r0 = f32x4{0.f, 0.f, 0.f, 0.f}; r1 = r0;
  f32x4 ss = r0, gg = r0;
#pragma unroll
  for (int ks = 0; ks < 14; ++ks) {
    if (ks + 2 < 14) {
      wf[(ks + 2) % 3][0] = *(const LAS v4u *)(wb_ + (ks + 2) * 1024);
      wf[(ks + 2) % 3][1] = *(const LAS v4u *)(wb_ + (16 + ks) * 1024);
    }
    SB();
    r0 = mfma32(wf[ks % 3][0], xf[ks], r0);
    r1 = mfma32(wf[ks % 3][1], xf[ks], r1);
    if (MODE == 2) {
      ss = mfma32(v4u{0x3f803f80u, 0x3f803f80u, 0x3f803f80u, 0x3f803f80u}, xf[ks], ss);
      gg = mfma32(xf[ks], xf[ks], gg);
      if (ks == jm) xk = xf[ks];
    }
    SB();
  }
  SB();
  if (MODE == 2) lc = ln_coef<true>(ss[0], gram_diag(gg, lane));
}
DI v4u add_res(v4u x, f32x4 a, f32x4 b) {
  v4u o;
  o.x = pk2(bflo(x.x) + rbf(a[0]), bfhi(x.x) + rbf(a[1])); o.y = pk2(bflo(x.y) + rbf(a[2]), bfhi(x.y) + rbf(a[3]));
  o.z = pk2(bflo(x.z) + rbf(b[0]), bfhi(x.z) + rbf(b[1])); o.w = pk2(bflo(x.w) + rbf(b[2]), bfhi(x.w) + rbf(b[3]));
  return o;
}

DI void dual_softmax(f32x4 p0, f32x4 p1, bool v1, v4u &pos, v4u &neg) {
  float pv[8];
#pragma unroll
  for (int i = 0; i < 4; ++i) { pv[i] = rbf(p0[i]) * LOG2E; pv[4 + i] = rbf(p1[i]) * LOG2E; }
  float mx = fmaxf(fmaxf(pv[0], pv[1]), fmaxf(pv[2], pv[3]));
  float mn = fminf(fminf(pv[0], pv[1]), fminf(pv[2], pv[3]));
  const float mx1 = fmaxf(fmaxf(pv[4], pv[5]), fmaxf(pv[6], pv[7]));
  const float mn1 = fminf(fminf(pv[4], pv[5]), fminf(pv[6], pv[7]));
  if (v1) { mx = fmaxf(mx, mx1); mn = fminf(mn, mn1); }
  mx = max_q(mx); mn = min_q(mn);
  float ep[8], en[8];
#pragma unroll
  for (int i = 0; i < 8; ++i) { ep[i] = ex2(pv[i] - mx); en[i] = ex2(mn - pv[i]); }
  float sp = (ep[0] + ep[1]) + (ep[2] + ep[3]), sn = (en[0] + en[1]) + (en[2] + en[3]);
  const float sp1 = (ep[4] + ep[5]) + (ep[6] + ep[7]), sn1 = (en[4] + en[5]) + (en[6] + en[7]);
  if (v1) { sp += sp1; sn += sn1; }
  sp = sum_q(sp); sn = sum_q(sn);
  const float rp = rcp(sp), rn = rcp(sn);
  pos.x = pk2(ep[0] * rp, ep[1] * rp); pos.y = pk2(ep[2] * rp, ep[3] * rp);
  neg.x = pk2(en[0] * rn, en[1] * rn); neg.y = pk2(en[2] * rn, en[3] * rn);
  pos.z = v1 ? pk2(ep[4] * rp, ep[5] * rp) : 0u; pos.w = v1 ? pk2(ep[6] * rp, ep[7] * rp) : 0u;
  neg.z = v1 ? pk2(en[4] * rn, en[5] * rn) : 0u; neg.w = v1 ? pk2(en[6] * rn, en[7] * rn) : 0u;
}
DI v2u trr(const LAS unsigned char *a) {
  return __builtin_bit_cast(v2u, __builtin_amdgcn_ds_read_tr16_b64_v4i16((LAS bf16x4 *)a));
}

__global__ void __launch_bounds__(NTHR, 2) k_main(Params p) {
  extern __shared__ __attribute__((aligned(16))) unsigned char smem[];
  Ctx c;
  c.rs = __builtin_amdgcn_make_buffer_rsrc((void *)p.ws, 0, WS_END, 0x00020000);
  c.ctrl = (gu32 *)p.ws;
  c.L = (LAS unsigned char *)smem;
  c.tid = threadIdx.x; c.lane = c.tid & 63; c.w = __builtin_amdgcn_readfirstlane(c.tid >> 6);
  const int b = blockIdx.x, xcd = b & 7, r = b >> 3;
  const int g = 4 * xcd + (r >> 3), j = r & 7;
  const int tau = 2 * xcd + (r >> 4), head = r & 15;
  const int w = c.w;
  const int jm = r & 15;
  const int jmc = jm < 14 ? jm : 0;
  const f32x4 z4 = {0.f, 0.f, 0.f, 0.f};

  dma_run<56>(p, c, WQKV_T + head * (84 * 1024) + 28 * 1024, L_WQ + 28 * 1024);
  v4u xf[14];
  {
    const int xb = opaque(XIN + (tau * 8 + w) * TILE_D);
#pragma unroll
    for (int ks = 0; ks < 14; ++ks) xf[ks] = LD16C(c.rs, c.lane * 16, xb, ks * 1024);
  }
  dma_run<28>(p, c, WQKV_T + head * (84 * 1024), L_WQ);
  SB();
  v4u cpf;
  {
    unsigned long long pc;
    asm volatile("s_getpc_b64 %0" : "=s"(pc));
    const int idx = (r * 8 + w) % CODE_PF;
    cpf = *(const GAS v4u *)((pc & ~255ull) + idx * 1024 + c.lane * 16);
  }

  for (int pair = 0; pair < 2; ++pair) {
    const unsigned e1 = pair + 1;
    const int xin = XIN + pair * ACT_D;
    v4u wr[28];
    const int w3b = W3_T + (16 * (j < 7 ? j : 0) + 2 * w) * 14 * 1024;
    f32x4 pq0, pq1;
    {
      FRESH();
      const v4u wph0 = ld16(c.rs, lane * 16, WPHI_T + (head * 2 + 0) * 1024);
      const v4u wph1 = ld16(c.rs, lane * 16, WPHI_T + (head * 2 + 1) * 1024);
      v4u cs[6];
#pragma unroll
      for (int nt = 0; nt < 6; ++nt) cs[nt] = ld16(c.rs, q4 * 16 + nt * 64, CS_QKV + head * 384);
      if (pair > 0) {
        DRAIN();
        wait2(c, C_XN + 2 * tau, C_XN + 2 * tau + 1, 7u * pair);
        const int xb = opaque(xin + (tau * 8 + w) * TILE_D);
#pragma unroll
        for (int ks = 0; ks < 14; ++ks) xf[ks] = LD16C(c.rs, lane * 16, xb, ks * 1024);
      }

      if (pair == 0) {
        DRAIN();
        __syncthreads();
        asm volatile("" :: "v"(cpf));
      }
      const LAS unsigned char *wb_ = c.L + L_WQ + lane * 16;
      f32x4 ss = z4, gg = z4;
      f32x4 acc[4];
#pragma unroll
      for (int nt = 0; nt < 4; ++nt) acc[nt] = z4;
      {
        v4u wf[2][4];
#pragma unroll
        for (int nt = 0; nt < 4; ++nt) wf[0][nt] = *(const LAS v4u *)(wb_ + ((2 + nt) * 14) * 1024);
        SB();
#pragma unroll
        for (int ks = 0; ks < 14; ++ks) {
          if (ks + 1 < 14) {
#pragma unroll
            for (int nt = 0; nt < 4; ++nt) wf[(ks + 1) & 1][nt] = *(const LAS v4u *)(wb_ + ((2 + nt) * 14 + ks + 1) * 1024);
          }
          SB();
#pragma unroll
          for (int nt = 0; nt < 4; ++nt) acc[nt] = mfma32(wf[ks & 1][nt], xf[ks], acc[nt]);
          ss = mfma32(v4u{0x3f803f80u, 0x3f803f80u, 0x3f803f80u, 0x3f803f80u}, xf[ks], ss);
          gg = mfma32(xf[ks], xf[ks], gg);
          SB();
        }
      }

      const LnC lc = ln_coef<false>(ss[0], gram_diag(gg, lane));
      const bool v1 = q4 < 3;
      {
        v2u kv_[4];
#pragma unroll
        for (int nt = 0; nt < 4; ++nt) {
          const f32x4 v = acc[nt] * lc.A - __builtin_bit_cast(f32x4, cs[2 + nt]) * lc.B;
          kv_[nt] = v2u{pk2(v[0], v[1]), pk2(v[2], v[3])};
        }
        const v4u kp = {kv_[0].x, kv_[0].y, kv_[1].x, kv_[1].y};
        v4u pp, pn;
        dual_softmax(mfma32(wph0, kp, z4), mfma32(wph1, kp, z4), v1, pp, pn);
        LAS unsigned char *pk = c.L + L_PK + (16 * w + t16) * PK_STR + q4 * 8;
        *(LAS v2u *)(pk) = v2u{pp.x, pp.y};
        *(LAS v2u *)(pk + 32) = v2u{pp.z, pp.w};
        *(LAS v2u *)(pk + 64) = v2u{pn.x, pn.y};
        *(LAS v2u *)(pk + 96) = v2u{pn.z, pn.w};
        LAS unsigned char *vv = c.L + L_V + (16 * w + t16) * V_STR + q4 * 8;
        *(LAS v2u *)(vv) = kv_[2];
        *(LAS v2u *)(vv + 32) = v1 ? kv_[3] : v2u{0x00003f80u, 0u};
      }
      __syncthreads();

      {
        const int ft = w >> 1, dt = w & 1;
        const int rq = (lane & 15) >> 2, rp = lane & 3;
        const LAS unsigned char *pa = c.L + L_PK + (8 * q4 + rq) * PK_STR + (16 * ft + 4 * rp) * 2;
        const LAS unsigned char *pb = c.L + L_V + (8 * q4 + rq) * V_STR + (16 * dt + 4 * rp) * 2;
        f32x4 kv = z4;
#pragma unroll
        for (int ks = 0; ks < 4; ++ks) {
          const v2u a0 = trr(pa + (32 * ks) * PK_STR), a1 = trr(pa + (32 * ks + 4) * PK_STR);
          const v2u b0 = trr(pb + (32 * ks) * V_STR), b1 = trr(pb + (32 * ks + 4) * V_STR);
          kv = mfma32(v4u{a0.x, a0.y, a1.x, a1.y}, v4u{b0.x, b0.y, b1.x, b1.y}, kv);
        }
        st8(c.rs, lane * 16 + (tau & 1) * 8, KVP_B + ((pair * 16 + head) * 8 + (tau >> 1)) * 8192 + w * 1024, v2u{pk2(kv[0], kv[1]), pk2(kv[2], kv[3])});
      }

      publish(c, C_KV + head);

      dma_run<28>(p, c, WO_T + jmc * (28 * 1024), L_WO);
      {
        f32x4 q0 = z4, q1 = z4;
        v4u wf[3][2];
#pragma unroll
        for (int ks = 0; ks < 2; ++ks) { wf[ks][0] = *(const LAS v4u *)(wb_ + ks * 1024); wf[ks][1] = *(const LAS v4u *)(wb_ + (14 + ks) * 1024); }
        SB();
#pragma unroll
        for (int ks = 0; ks < 14; ++ks) {
          if (ks + 2 < 14) {
            wf[(ks + 2) % 3][0] = *(const LAS v4u *)(wb_ + (ks + 2) * 1024);
            wf[(ks + 2) % 3][1] = *(const LAS v4u *)(wb_ + (16 + ks) * 1024);
          }
          SB();
          q0 = mfma32(wf[ks % 3][0], xf[ks], q0);
          q1 = mfma32(wf[ks % 3][1], xf[ks], q1);
          SB();
        }
        q0 = q0 * lc.A - __builtin_bit_cast(f32x4, cs[0]) * lc.B;
        q1 = q1 * lc.A - __builtin_bit_cast(f32x4, cs[1]) * lc.B;
        const v4u qp = {pk2(q0[0], q0[1]), pk2(q0[2], q0[3]), pk2(q1[0], q1[1]), pk2(q1[2], q1[3])};
        pq0 = mfma32(wph0, qp, z4); pq1 = mfma32(wph1, qp, z4);
      }
#pragma unroll
      for (int ks = 0; ks < 14; ++ks) xf[ks] = v4u{0u, 0u, 0u, 0u};
    }
    {
      FRESH();
      wait1(c, C_KV + head, 16u * e1);

      v4u pr[8];
      const int kb = opaque(KVP_B + (pair * 16 + head) * 8 * 8192 + w * 1024);
#pragma unroll
      for (int t = 0; t < 8; ++t) pr[t] = ld16(c.rs, lane * 16, kb + t * 8192);
      SB();
      v4u phq_p, phq_n;
      dual_softmax(pq0, pq1, q4 < 3, phq_p, phq_n);
      SB();
      f32x4 s = z4;
#pragma unroll
      for (int t = 0; t < 8; ++t) {
        s += f32x4{bflo(pr[t].x), bfhi(pr[t].x), bflo(pr[t].y), bfhi(pr[t].y)};
        s += f32x4{bflo(pr[t].z), bfhi(pr[t].z), bflo(pr[t].w), bfhi(pr[t].w)};
      }
      {
        const int ft = w >> 1, dt = w & 1, d = 16 * dt + t16, part = ft >> 1, half = ft & 1;
        *(LAS v2u *)(c.L + L_KVS + ((d * 2 + part) * 4 + q4) * 16 + half * 8) = v2u{pk2(s[0], s[1]), pk2(s[2], s[3])};
      }
      __syncthreads();
      f32x4 nm[2];
#pragma unroll
      for (int dt = 0; dt < 2; ++dt) {
        const v4u ap = *(const LAS v4u *)(c.L + L_KVS + (((16 * dt + t16) * 2 + 0) * 4 + q4) * 16);
        const v4u an = *(const LAS v4u *)(c.L + L_KVS + (((16 * dt + t16) * 2 + 1) * 4 + q4) * 16);
        nm[dt] = mfma32(ap, phq_p, z4);
        nm[dt] = mfma32(an, phq_n, nm[dt]);
      }
      const float den0 = __shfl(nm[1][0], 48 + t16);
      const float rden = rcp(rbf(rbf(den0) + 1e-6f));
#pragma unroll
      for (int dt = 0; dt < 2; ++dt) {
        if (dt == 0 || q4 < 3) {
          const int col = DH * head + 16 * dt + 4 * q4;
          v2u o;
          o.x = pk2(rbf(nm[dt][0]) * rden, rbf(nm[dt][1]) * rden);
          o.y = pk2(rbf(nm[dt][2]) * rden, rbf(nm[dt][3]) * rden);
          st8(c.rs, (col >> 3) * 256 + t16 * 16 + ((col >> 2) & 1) * 8, O_B + pair * ACT_D + (tau * 8 + w) * TILE_D, o);
        }
      }

      publish(c, C_O + tau);

      dma_run<28>(p, c, W1_T + jmc * (28 * 1024), L_W1);
      dma_run<28>(p, c, W2_T + jmc * (28 * 1024), L_W2);
    }
    if (jm < 14) {
      const int tile = tau * 8 + w;
      v4u yk;
      {
        FRESH();
        const int eo = tile * TILE_D + jm * 1024;
        const v4u xr = ld16(c.rs, lane * 16, xin + eo);
        wait1(c, C_O + tau, 16u * e1);
        f32x4 r0, r1; LnC lc; v4u xk;
        gemm_dir<0>(c, O_B + pair * ACT_D, tile, L_WO, jm, r0, r1, lc, xk);

        st16(c.rs, lane * 16, XP_B + pair * ACT_D + eo, add_res(xr, r0, r1));
        publish(c, C_XP + tau);
        load_run<0, 10, 28>(c, w3b, wr);
      }
      {
        FRESH();
        const int eo = tile * TILE_D + jm * 1024;
        const f32x4 cs0 = __builtin_bit_cast(f32x4, ld16(c.rs, q4 * 32, CS_W1 + jm * 128));
        const f32x4 cs1 = __builtin_bit_cast(f32x4, ld16(c.rs, q4 * 32 + 16, CS_W1 + jm * 128));
        wait1(c, C_XP + tau, 14u * e1);
        f32x4 r0, r1; LnC lc; v4u xk;
        gemm_dir<2>(c, XP_B + pair * ACT_D, tile, L_W1, jm, r0, r1, lc, xk);

        r0 = r0 * lc.A - cs0 * lc.B;
        r1 = r1 * lc.A - cs1 * lc.B;
        {
          const int k8 = 4 * jm + q4, cg = k8 / 14;
          const int d = (cg == 0) ? 1 : (cg == 1) ? -1 : (cg == 2) ? 2 : -2;
          const int tok = 16 * tile + t16, sl = tok + d;
          const int ho = (sl < 0 || sl >= S_) ? 0x7ff00000 : (sl >> 4) * TILE_D + k8 * 256 + (sl & 15) * 16;
          st16(c.rs, ho, H_B + pair * ACT_D, v4u{gelu2(r0[0], r0[1]), gelu2(r0[2], r0[3]), gelu2(r1[0], r1[1]), gelu2(r1[2], r1[3])});
          const int src = tok - d;
          if (src < 0 || src >= S_) st16(c.rs, (tok >> 4) * TILE_D + k8 * 256 + (tok & 15) * 16, H_B + pair * ACT_D, v4u{0u, 0u, 0u, 0u});
        }
#pragma unroll
        for (int e = 0; e < 4; ++e) {
          const unsigned d = pk2(bflo(xk[e]) - lc.mb, bfhi(xk[e]) - lc.mb);
          yk[e] = pk2(bflo(d) * lc.r, bfhi(d) * lc.r);
        }
        publish(c, C_H + tau);
        load_run<10, 19, 28>(c, w3b, wr);
      }
      {
        FRESH();
        const int eo = tile * TILE_D + jm * 1024;
        wait3(c, C_H + tau, C_H + (tau > 0 ? tau - 1 : tau), C_H + (tau < 15 ? tau + 1 : tau), 14u * e1);
        f32x4 r0, r1; LnC lc; v4u xk;
        gemm_dir<0>(c, H_B + pair * ACT_D, tile, L_W2, jm, r0, r1, lc, xk);

        st16(c.rs, lane * 16, X1_B + pair * ACT_D + eo, add_res(yk, r0, r1));
        publish(c, C_X1 + tau);
        load_run<19, 28, 28>(c, w3b, wr);
      }
    } else {
      FRESH();
      load_run<0, 28, 28>(c, w3b, wr);
    }
    if (j < 7) {
      {
        FRESH();
        const f32x4 cs0 = __builtin_bit_cast(f32x4, ld16(c.rs, q4 * 32, CS_W3 + (256 * j + 32 * w) * 4));
        const f32x4 cs1 = __builtin_bit_cast(f32x4, ld16(c.rs, q4 * 32 + 16, CS_W3 + (256 * j + 32 * w) * 4));
        wait1(c, C_X1 + (g >> 1), 14u * e1);
        {
          v4u a[7];
          stage_issue(c, X1_B + pair * ACT_D, g, a);
          stage_finish(c, a);
        }
        f32x4 acc[4][2];
#pragma unroll
        for (int m = 0; m < 4; ++m) { acc[m][0] = z4; acc[m][1] = z4; }
        const int w4b = opaque(W4_T + (4 * j * 56 + 7 * w) * 1024);
        {
          v4u bf[2][4];
          const LAS unsigned char *ab = c.L + L_A + lane * 16;
#pragma unroll
          for (int m = 0; m < 4; ++m) bf[0][m] = *(const LAS v4u *)(ab + m * TILE_D);
#pragma unroll
          for (int ks = 0; ks < 14; ++ks) {
            if (ks + 1 < 14) {
#pragma unroll
              for (int m = 0; m < 4; ++m) bf[(ks + 1) & 1][m] = *(const LAS v4u *)(ab + m * TILE_D + (ks + 1) * 1024);
            }
            SB();
#pragma unroll
            for (int m = 0; m < 4; ++m) {
              acc[m][0] = mfma32(wr[ks], bf[ks & 1][m], acc[m][0]);
              acc[m][1] = mfma32(wr[14 + ks], bf[ks & 1][m], acc[m][1]);
            }
            SB();
            wr[ks] = LD16C(c.rs, lane * 16, w4b, ((ks / 7) * 56 + ks % 7) * 1024);
            wr[14 + ks] = LD16C(c.rs, lane * 16, w4b, ((2 + ks / 7) * 56 + ks % 7) * 1024);
          }
          SB();
        }
#pragma unroll
        for (int m = 0; m < 4; ++m) {
          const f32x2 st = *(const LAS f32x2 *)(c.L + L_ST + (16 * m + t16) * 8);
          const f32x4 u0 = acc[m][0] * st.x - cs0 * st.y, u1 = acc[m][1] * st.x - cs1 * st.y;
          const v4u o = {gelu2(u0[0], u0[1]), gelu2(u0[2], u0[3]), gelu2(u1[0], u1[1]), gelu2(u1[2], u1[3])};
          st16(c.rs, lane * 16, G_B + pair * ACT_F + (4 * g + m) * TILE_F + (32 * j + 4 * w) * 256, o);
        }
        publish(c, C_G + g);
      }
      {
        FRESH();
        const int roff = q4 * TILE_D + t16 * 16;
        const int rso = 4 * g * TILE_D + (8 * j + w) * 256;
        const v4u rxp = ld16(c.rs, roff, XP_B + pair * ACT_D + rso);
        const v4u rx1 = ld16(c.rs, roff, X1_B + pair * ACT_D + rso);
        wait1(c, C_G + g, 7u * e1);
        const int abase = opaque(G_B + pair * ACT_F + 4 * g * TILE_F + 7 * w * 1024);
        constexpr int RING = 14;
        v4u a[RING];
#pragma unroll
        for (int i = 0; i < RING; ++i) a[i] = LD16C(c.rs, lane * 16, abase, (i / 7) * TILE_F + (i % 7) * 1024);
        SB();
#pragma unroll
        for (int m = 0; m < 4; ++m) {
          f32x4 acc[4];
#pragma unroll
          for (int t = 0; t < 4; ++t) acc[t] = z4;
#pragma unroll
          for (int kk = 0; kk < 7; ++kk) {
            const int i = m * 7 + kk;
            const v4u cur = a[i % RING];
            if (i + RING < 28) a[i % RING] = LD16C(c.rs, lane * 16, abase, ((i + RING) / 7) * TILE_F + ((i + RING) % 7) * 1024);
#pragma unroll
            for (int t = 0; t < 4; ++t) acc[t] = mfma32(wr[t * 7 + kk], cur, acc[t]);
            SB();
          }
#pragma unroll
          for (int t = 0; t < 4; ++t) *(LAS f32x4 *)(c.L + L_RED + ((w * 16 + m * 4 + t) * 64 + lane) * 16) = acc[t];
        }
        if (pair == 0) {
          const int qb = opaque(WQKV_T + head * (84 * 1024) + w * 1024);
#pragma unroll
          for (int i = 0; i < 11; ++i) wr[i] = ld16(c.rs, lane * 16, qb + (w + 8 * i < 84 ? i * 8192 : 0));
          SB();
        }

        __syncthreads();
        f32x4 s0 = z4, s1 = z4;
        {
          const int t = w >> 1, qq = 2 * (w & 1);
#pragma unroll
          for (int ww = 0; ww < 8; ++ww) {
            s0 += *(const LAS f32x4 *)(c.L + L_RED + ((ww * 16 + q4 * 4 + t) * 64 + t16 + 16 * qq) * 16);
            s1 += *(const LAS f32x4 *)(c.L + L_RED + ((ww * 16 + q4 * 4 + t) * 64 + t16 + 16 * (qq + 1)) * 16);
          }
        }
        v4u o;
        o.x = pk2(bflo(rxp.x) + rbf(bflo(rx1.x) + rbf(s0[0])), bfhi(rxp.x) + rbf(bfhi(rx1.x) + rbf(s0[1])));
        o.y = pk2(bflo(rxp.y) + rbf(bflo(rx1.y) + rbf(s0[2])), bfhi(rxp.y) + rbf(bfhi(rx1.y) + rbf(s0[3])));
        o.z = pk2(bflo(rxp.z) + rbf(bflo(rx1.z) + rbf(s1[0])), bfhi(rxp.z) + rbf(bfhi(rx1.z) + rbf(s1[1])));
        o.w = pk2(bflo(rxp.w) + rbf(bflo(rx1.w) + rbf(s1[2])), bfhi(rxp.w) + rbf(bfhi(rx1.w) + rbf(s1[3])));

        if (pair == 0) {
          st16(c.rs, roff, XIN + ACT_D + rso, o);
          publish(c, C_XN + g);
#pragma unroll
          for (int i = 0; i < 11; ++i)
            if (w + 8 * i < 84) *(LAS v4u *)(c.L + L_WQ + (w + 8 * i) * 1024 + lane * 16) = wr[i];
        } else {
          *(v4u *)(p.out + (size_t)(64 * g + 16 * q4 + t16) * D_ + 64 * j + 8 * w) = o;
        }

      }
    }
    else if (pair == 0) {
      DRAIN();
      __syncthreads();
      c.lane = opaque_v(c.lane);
      dma_run<84>(p, c, WQKV_T + head * (84 * 1024), L_WQ);
    }
  }
}

}

extern "C" void kernel_launch(void *const *d_in, const int *in_sizes, int n_in, void *d_out, int out_size, void *d_ws,
                              size_t ws_size, hipStream_t stream) {
  Params p;
  p.x = (const unsigned short *)d_in[0];
  p.wqkv = (const unsigned short *)d_in[1];
  p.wphi = (const unsigned short *)d_in[2];
  p.wo = (const unsigned short *)d_in[3];
  p.w1 = (const unsigned short *)d_in[5];
  p.w2 = (const unsigned short *)d_in[6];
  p.w3 = (const unsigned short *)d_in[7];
  p.w4 = (const unsigned short *)d_in[8];
  p.out = (unsigned short *)d_out;
  p.ws = (unsigned char *)d_ws;
  (void)hipFuncSetAttribute((const void *)k_main, hipFuncAttributeMaxDynamicSharedMemorySize, LDS_BYTES);
  hipLaunchKernelGGL(k_prep, dim3(PREP_BLOCKS), dim3(256 * PJ), 0, stream, p);
  hipLaunchKernelGGL(k_main, dim3(256), dim3(NTHR), LDS_BYTES, stream, p);
}
```

```cpp
#include <hip/hip_runtime.h>
#include <hip/hip_bf16.h>
#include <stdint.h>

#define GAS __attribute__((address_space(1)))
#define LAS __attribute__((address_space(3)))
#define RLX_AGENT __ATOMIC_RELAXED, __HIP_MEMORY_SCOPE_AGENT
#define DI __device__ __forceinline__
typedef unsigned v4u __attribute__((ext_vector_type(4)));
typedef unsigned v2u __attribute__((ext_vector_type(2)));
typedef float f32x4 __attribute__((ext_vector_type(4)));
typedef float f32x2 __attribute__((ext_vector_type(2)));
typedef short bf16x8 __attribute__((ext_vector_type(8)));
typedef short bf16x4 __attribute__((ext_vector_type(4)));
typedef __bf16 bfv2 __attribute__((ext_vector_type(2)));
typedef GAS unsigned gu32;

namespace {

constexpr int S_ = 2048, D_ = 448, DH = 28, F_ = 1792;
constexpr int NTHR = 512;
constexpr int TILE_D = (D_ / 8) * 256;
constexpr int TILE_F = (F_ / 8) * 256;
constexpr int ACT_D = 128 * TILE_D;
constexpr int ACT_F = 128 * TILE_F;

constexpr int CSTR = 64;
constexpr int C_XN = 1;
constexpr int C_KV = C_XN + 32;
constexpr int C_O = C_KV + 16;
constexpr int C_XP = C_O + 16;
constexpr int C_H = C_XP + 16;
constexpr int C_X1 = C_H + 16;
constexpr int C_G = C_X1 + 16;
constexpr int C_NCTR = C_G + 32;
constexpr int CTRL_BYTES = ((C_NCTR * CSTR * 4 + 65535) / 65536) * 65536;
static_assert(C_NCTR * CSTR * 4 <= CTRL_BYTES, "control block");
constexpr int WQKV_T = CTRL_BYTES;
constexpr int WO_T = WQKV_T + 96 * 14 * 1024;
constexpr int W1_T = WO_T + 28 * 14 * 1024;
constexpr int W2_T = W1_T + 28 * 14 * 1024;
constexpr int W3_T = W2_T + 28 * 14 * 1024;
constexpr int W4_T = W3_T + 112 * 14 * 1024;
constexpr int WPHI_T = W4_T + 28 * 56 * 1024;
constexpr int CS_QKV = WPHI_T + 16 * 2 * 1024;
constexpr int CS_W1 = CS_QKV + 96 * 16 * 4;
constexpr int CS_W3 = CS_W1 + 448 * 4;
constexpr int W_END = CS_W3 + 1792 * 4;
constexpr int XIN = (W_END + 4095) & ~4095;
constexpr int O_B = XIN + 2 * ACT_D;
constexpr int XP_B = O_B + 2 * ACT_D;
constexpr int H_B = XP_B + 2 * ACT_D;
constexpr int X1_B = H_B + 2 * ACT_D;
constexpr int G_B = X1_B + 2 * ACT_D;
constexpr int KVP_B = G_B + 2 * ACT_F;
constexpr int WS_END = KVP_B + 2 * 16 * 16 * 8192;

constexpr int L_A = 0;
constexpr int L_ST = 57344;
constexpr int L_RED = 0;
constexpr int L_WQ = 65536;
constexpr int PK_STR = 136, V_STR = 72;
constexpr int L_PK = 0;
constexpr int L_V = 17408;
constexpr int L_KVS = 26624;
constexpr int L_WO = 32768;
constexpr int L_W1 = 65536;
constexpr int L_W2 = 94208;
constexpr int LDS_BYTES = 151552;

constexpr int CODE_PF = 18;
constexpr unsigned kSpinCap = 1u << 22;

struct Params {
  const unsigned short *x, *wqkv, *wphi, *wo, *w1, *w2, *w3, *w4;
  unsigned short *out;
  unsigned char *ws;
};

DI float asf(unsigned u) { return __builtin_bit_cast(float, u); }
DI unsigned asu(float f) { return __builtin_bit_cast(unsigned, f); }
DI float bflo(unsigned u) { return asf(u << 16); }
DI float bfhi(unsigned u) { return asf(u & 0xffff0000u); }
DI unsigned pk2(float a, float b) {
  f32x2 v = {a, b};
  return __builtin_bit_cast(unsigned, __builtin_convertvector(v, bfv2));
}
DI float rbf(float a) { return (float)(__bf16)a; }
DI float rcp(float a) { return __builtin_amdgcn_rcpf(a); }
DI float ex2(float a) { return __builtin_amdgcn_exp2f(a); }
DI float dot2(unsigned a, unsigned b, float c) {
  return __builtin_amdgcn_fdot2_f32_bf16(__builtin_bit_cast(bfv2, a), __builtin_bit_cast(bfv2, b), c, false);
}
DI f32x4 mfma32(v4u a, v4u b, f32x4 c) {
  return __builtin_amdgcn_mfma_f32_16x16x32_bf16(__builtin_bit_cast(bf16x8, a), __builtin_bit_cast(bf16x8, b), c, 0, 0, 0);
}
#define DRAIN() asm volatile("s_waitcnt vmcnt(0)" ::: "memory")
constexpr int SC1 = 16;
DI v4u ld16(__amdgpu_buffer_rsrc_t rs, int voff, int soff) { return __builtin_amdgcn_raw_buffer_load_b128(rs, voff, soff, SC1); }
DI v2u ld8(__amdgpu_buffer_rsrc_t rs, int voff, int soff) { return __builtin_amdgcn_raw_buffer_load_b64(rs, voff, soff, SC1); }
DI void st16(__amdgpu_buffer_rsrc_t rs, int voff, int soff, v4u v) { __builtin_amdgcn_raw_buffer_store_b128(v, rs, voff + soff, 0, SC1); }
DI void st8(__amdgpu_buffer_rsrc_t rs, int voff, int soff, v2u v) { __builtin_amdgcn_raw_buffer_store_b64(v, rs, voff, soff, SC1); }
DI void st4(__amdgpu_buffer_rsrc_t rs, int voff, int soff, unsigned v) { __builtin_amdgcn_raw_buffer_store_b32(v, rs, voff, soff, SC1); }
#define LD16C(rs, voff, soff, co) ld16(rs, (voff) + ((co) & 4095), (soff) + ((co) & ~4095))
DI int opaque(int x) { asm volatile("" : "+s"(x)); return x; }
DI int opaque_v(int x) { asm volatile("" : "+v"(x)); return x; }
#define FRESH() c.lane = opaque_v(c.lane); const int lane = c.lane, t16 = lane & 15, q4 = lane >> 4; (void)t16; (void)q4;
#define SB() __builtin_amdgcn_sched_barrier(0)

template <int CTRL> DI float dppf(float v) {
  return asf(__builtin_amdgcn_update_dpp(0, (int)asu(v), CTRL, 0xf, 0xf, false));
}
DI float sum_q(float v) {
  auto r = __builtin_amdgcn_permlane16_swap(asu(v), asu(v), false, false);
  v = asf(r[0]) + asf(r[1]);
  auto s = __builtin_amdgcn_permlane32_swap(asu(v), asu(v), false, false);
  return asf(s[0]) + asf(s[1]);
}
DI float max_q(float v) {
  auto r = __builtin_amdgcn_permlane16_swap(asu(v), asu(v), false, false);
  v = fmaxf(asf(r[0]), asf(r[1]));
  auto s = __builtin_amdgcn_permlane32_swap(asu(v), asu(v), false, false);
  return fmaxf(asf(s[0]), asf(s[1]));
}
DI float min_q(float v) {
  auto r = __builtin_amdgcn_permlane16_swap(asu(v), asu(v), false, false);
  v = fminf(asf(r[0]), asf(r[1]));
  auto s = __builtin_amdgcn_permlane32_swap(asu(v), asu(v), false, false);
  return fminf(asf(s[0]), asf(s[1]));
}

constexpr float LOG2E = 1.4426950408889634f;
DI unsigned gelu2(float a, float b) {
  const f32x2 x = {a, b};
  constexpr float c0 = -2.0f * 0.7978845608028654f * LOG2E, c1 = c0 * 0.044715f;
  const f32x2 u = x * (x * x * c1 + c0);
  f32x2 e = {ex2(u.x), ex2(u.y)};
  e = e + 1.0f;
  const f32x2 r = {rcp(e.x), rcp(e.y)};
  const f32x2 o = x * r;
  return pk2(o.x, o.y);
}

DI void poll_ge(gu32 *w, unsigned want, gu32 *tmo) {
#pragma nounroll
  for (unsigned spins = 0;;) {
    if (__hip_atomic_load(w, RLX_AGENT) >= want) return;
    __builtin_amdgcn_s_sleep(1);
    if (++spins > kSpinCap) { __hip_atomic_store(tmo, 1u, RLX_AGENT); return; }
    if ((spins & 1023u) == 0u && __hip_atomic_load(tmo, RLX_AGENT) != 0u) return;
  }
}

struct LnC { float A, B, mb, r; };
template <bool TWICE> DI LnC ln_coef(float s1, float s2) {
  const float mean = s1 * (1.0f / D_);
  const float var = fmaxf(s2 * (1.0f / D_) - mean * mean, 0.f);
  LnC o;
  o.mb = rbf(mean);
  const float sd = rbf(__builtin_amdgcn_sqrtf(rbf(rbf(var) + 1e-5f)));
  o.r = rcp(sd);
  if (TWICE) {
    const float my = (mean - o.mb) * o.r, vy = var * o.r * o.r;
    const float mby = rbf(my);
    const float ry = rcp(rbf(__builtin_amdgcn_sqrtf(rbf(rbf(vy) + 1e-5f))));
    o.A = o.r * ry;
    o.B = (o.mb * o.r + mby) * ry;
  } else {
    o.A = o.r;
    o.B = o.mb * o.r;
  }
  return o;
}

DI float gram_diag(f32x4 gg, int lane) {
  const int t16 = lane & 15, q4 = lane >> 4;
  const float a = (t16 & 1) ? gg[1] : gg[0], b = (t16 & 1) ? gg[3] : gg[2];
  const float d = (t16 & 2) ? b : a;
  return sum_q(q4 == (t16 >> 2) ? d : 0.f);
}

constexpr int NB_X = S_ * (D_ / 8) / 256;
constexpr int NJ_Q = 3 * 4 * 14;
constexpr int NJ_S = 3 * 7 * 14, NJ_3 = 28 * 14, NJ_4 = 7 * 56;
constexpr int NB_W = NJ_Q + NJ_S + NJ_3 + NJ_4;
constexpr int NB_PHI = 8;
constexpr int NB_CS = 21 + 7 + 28;
constexpr int PREP_BLOCKS = NB_W + NB_CS + NB_PHI + NB_X;

__global__ void __launch_bounds__(256) k_prep(Params p) {
  __shared__ __attribute__((aligned(16))) unsigned char sm[32 * 240];
  f32x4 *red = (f32x4 *)sm;
  const __amdgpu_buffer_rsrc_t rs = __builtin_amdgcn_make_buffer_rsrc((void *)p.ws, 0, WS_END, 0x00020000);
  const int tid = threadIdx.x;
  int b = blockIdx.x < NB_CS + NB_PHI ? NB_W + blockIdx.x : (blockIdx.x < NB_CS + NB_PHI + NB_W ? blockIdx.x - (NB_CS + NB_PHI) : blockIdx.x);
  if (b < NJ_Q) {
    const int ks = b % 14, hg = (b / 14) & 3, part = b / 56;
    const unsigned short *src = p.wqkv + (size_t)(ks * 32) * (3 * D_) + part * D_ + hg * 112;
#pragma unroll
    for (int i = 0; i < 2; ++i) {
      const int ch = tid + 256 * i;
      if (ch < 448) {
        const int row = ch / 14, cc = ch % 14;
        *(v4u *)(sm + row * 240 + cc * 16) = *(const v4u *)(src + (size_t)row * (3 * D_) + cc * 8);
      }
    }
    __syncthreads();
#pragma unroll
    for (int i = 0; i < 2; ++i) {
      const int f = (tid >> 6) + 4 * i, slot = tid & 63, r = slot & 15, kg = slot >> 4;
      const int cl = 16 * (f & 1) + r;
      const bool valid = cl < DH;
      const unsigned short *q_ = (const unsigned short *)(sm + (8 * kg) * 240) + 28 * (f >> 1) + (valid ? cl : 0);
      v4u o;
#pragma unroll
      for (int jj = 0; jj < 4; ++jj) o[jj] = valid ? (unsigned)q_[(2 * jj) * 120] | ((unsigned)q_[(2 * jj + 1) * 120] << 16) : 0u;
      const int nt = (4 * hg + (f >> 1)) * 6 + part * 2 + (f & 1);
      st16(rs, WQKV_T + (nt * 14 + ks) * 1024 + slot * 16, 0, o);
    }
    return;
  }
  if (b < NB_W) {
    int jb = b - NJ_Q;
    const unsigned short *src; int ld, dst, cb, ks, KS; bool perm = true;
    if (jb < NJ_S) {
      const int m = jb / 98, rr = jb % 98;
      cb = rr / 14; ks = rr % 14; KS = 14;
      src = m == 0 ? p.wo : (m == 1 ? p.w1 : p.w2); ld = D_; dst = WO_T + m * (28 * 14 * 1024);
    } else if (jb < NJ_S + NJ_3) {
      jb -= NJ_S; cb = jb / 14; ks = jb % 14; KS = 14; src = p.w3; ld = F_; dst = W3_T;
    } else {
      jb -= NJ_S + NJ_3; cb = jb / 56; ks = jb % 56; KS = 56; src = p.w4; ld = D_; dst = W4_T; perm = false;
    }
    {
      const int row = tid >> 3, cc = tid & 7;
      *(v4u *)(sm + row * 144 + cc * 16) = *(const v4u *)(src + (size_t)(ks * 32 + row) * ld + cb * 64 + cc * 8);
    }
    __syncthreads();
    const int f = tid >> 6, slot = tid & 63, r = slot & 15, kg = slot >> 4;
    const int cl = perm ? 32 * (f >> 1) + 8 * (r >> 2) + 4 * (f & 1) + (r & 3) : 16 * f + r;
    const unsigned short *q_ = (const unsigned short *)(sm + (8 * kg) * 144) + cl;
    v4u o;
#pragma unroll
    for (int jj = 0; jj < 4; ++jj) o[jj] = (unsigned)q_[(2 * jj) * 72] | ((unsigned)q_[(2 * jj + 1) * 72] << 16);
    st16(rs, dst + ((4 * cb + f) * KS + ks) * 1024 + slot * 16, 0, o);
    return;
  }
  b -= NB_W;
  if (b < NB_CS) {
    const unsigned short *src; int ld, col0;
    if (b < 21) { src = p.wqkv; ld = 3 * D_; col0 = b * 64; }
    else if (b < 28) { src = p.w1; ld = D_; col0 = (b - 21) * 64; }
    else { src = p.w3; ld = F_; col0 = (b - 28) * 64; }
    const int n4 = tid & 15, ksl = tid >> 4;
    const unsigned short *s0 = src + (size_t)(ksl * 28) * ld + col0 + 4 * n4;
    f32x4 s = {0.f, 0.f, 0.f, 0.f};
    v2u e[28];
#pragma unroll
    for (int k = 0; k < 28; ++k) e[k] = *(const v2u *)(s0 + (size_t)k * ld);
    SB();
#pragma unroll
    for (int k = 0; k < 28; ++k) s += f32x4{bflo(e[k].x), bfhi(e[k].x), bflo(e[k].y), bfhi(e[k].y)};
    red[tid] = s;
    __syncthreads();
    if (tid < 64) {
      float a = 0.f;
#pragma unroll
      for (int k = 0; k < 16; ++k) a += ((const float *)red)[(k * 16 + (tid >> 2)) * 4 + (tid & 3)];
      const int col = col0 + tid;
      int off;
      if (b < 21) {
        const int part = col / D_, rem = col % D_, head = rem / DH, cc = rem % DH;
        off = CS_QKV + ((head * 6 + part * 2 + (cc >> 4)) * 16 + (cc & 15)) * 4;
      } else if (b < 28) off = CS_W1 + col * 4;
      else off = CS_W3 + col * 4;
      st4(rs, off, 0, asu(a));
    } else if (b == 0 && tid >= 64) {
      const int i = tid - 64;
      if (i < 192) st4(rs, CS_QKV + (((i >> 2) * 2 + 1) * 16 + 12 + (i & 3)) * 4, 0, 0u);
    }
    return;
  }
  b -= NB_CS;
  if (b < NB_PHI) {
    const int pid = b * 256 + tid, fr = pid >> 6, lane = pid & 63, r = lane & 15, kg = lane >> 4;
    const int h = fr >> 1, f = (fr & 1) * 16 + r;
    unsigned short e[8];
#pragma unroll
    for (int j = 0; j < 8; ++j) {
      const int d = (j < 4) ? 4 * kg + j : 16 + 4 * kg + (j - 4);
      e[j] = (d < DH && f < DH) ? p.wphi[(h * DH + d) * DH + f] : (unsigned short)0;
    }
    v4u v;
    v.x = e[0] | ((unsigned)e[1] << 16); v.y = e[2] | ((unsigned)e[3] << 16);
    v.z = e[4] | ((unsigned)e[5] << 16); v.w = e[6] | ((unsigned)e[7] << 16);
    st16(rs, WPHI_T + pid * 16, 0, v);
    if (pid < C_NCTR) __hip_atomic_store((gu32 *)p.ws + CSTR * pid, 0u, RLX_AGENT);
    return;
  }
  b -= NB_PHI;
  {
    const int fr = b * 4 + (tid >> 6), tile = fr / 14, ks = fr % 14, lane = tid & 63;
    const v4u v = *(const v4u *)(p.x + (size_t)(tile * 16 + (lane & 15)) * D_ + ks * 32 + (lane >> 4) * 8);
    st16(rs, XIN + tile * TILE_D + ks * 1024 + lane * 16, 0, v);
  }
}

struct Ctx {
  __amdgpu_buffer_rsrc_t rs;
  gu32 *ctrl;
  LAS unsigned char *L;
  int tid, lane, w;
};

DI void wait1(const Ctx &c, int ctr, unsigned want) {
  if (c.tid == NTHR - 64) {
    poll_ge(c.ctrl + CSTR * ctr, want, c.ctrl);
    __builtin_amdgcn_fence(__ATOMIC_ACQUIRE, "wavefront");
  }
  __syncthreads();
}
DI void wait2(const Ctx &c, int c0, int c1, unsigned want) {
  if (c.tid >= NTHR - 64 && c.tid < NTHR - 62) {
    poll_ge(c.ctrl + CSTR * (c.tid == NTHR - 64 ? c0 : c1), want, c.ctrl);
    __builtin_amdgcn_fence(__ATOMIC_ACQUIRE, "wavefront");
  }
  __syncthreads();
}
DI void wait3(const Ctx &c, int c0, int c1, int c2, unsigned want) {
  if (c.tid >= NTHR - 64 && c.tid < NTHR - 61) {
    poll_ge(c.ctrl + CSTR * (c.tid == NTHR - 64 ? c0 : c.tid == NTHR - 63 ? c1 : c2), want, c.ctrl);
    __builtin_amdgcn_fence(__ATOMIC_ACQUIRE, "wavefront");
  }
  __syncthreads();
}
DI void publish(const Ctx &c, int ctr) {
  DRAIN();
  __syncthreads();
  if (c.tid == 0) __hip_atomic_fetch_add(c.ctrl + CSTR * ctr, 1u, RLX_AGENT);
}

DI void dma1k(const unsigned char *gsrc, unsigned lds_dst) {
  unsigned keep;
  asm volatile("s_mov_b32 %0, m0\n\ts_mov_b32 m0, %2\n\ts_nop 0\n\tglobal_load_lds_dwordx4 %1, off sc1\n\ts_mov_b32 m0, %0"
               : "=&s"(keep) : "v"(gsrc), "s"(lds_dst) : "memory");
}
template <int NFR> DI void dma_run(const Params &p, const Ctx &c, int src, int dst) {
  const unsigned char *g = p.ws + src + c.w * 1024 + c.lane * 16;
  const unsigned l = (unsigned)(size_t)c.L + dst + c.w * 1024;
#pragma unroll
  for (int i = 0; i < (NFR + 7) / 8; ++i) {
    if (c.w + 8 * i < NFR) dma1k(g + i * 8192, l + i * 8192);
  }
}

template <int LO, int HI, int N> DI void load_run(const Ctx &c, int base, v4u (&w)[N]) {
  base = opaque(base);
#pragma unroll
  for (int i = LO; i < HI; ++i) w[i] = LD16C(c.rs, c.lane * 16, base, i * 1024);
  SB();
}

DI void stage_issue(const Ctx &c, int srcbase, int g, v4u (&a)[7]) {
  srcbase = opaque(srcbase + 4 * g * TILE_D);
  const int tt = 8 * c.w + (c.lane & 7), kq = c.lane >> 3;
#pragma unroll
  for (int i = 0; i < 7; ++i) a[i] = LD16C(c.rs, (tt >> 4) * TILE_D + kq * 256 + (tt & 15) * 16, srcbase, i * 2048);
  SB();
}
DI void stage_finish(const Ctx &c, v4u (&a)[7]) {
  const int tt = 8 * c.w + (c.lane & 7), kq = c.lane >> 3;
#pragma unroll
  for (int i = 0; i < 7; ++i)
    *(LAS v4u *)(c.L + L_A + (tt >> 4) * TILE_D + (kq + 8 * i) * 256 + (tt & 15) * 16) = a[i];
  {
    float s1a = 0.f, s1b = 0.f, s2a = 0.f, s2b = 0.f;
#pragma unroll
    for (int i = 0; i < 7; ++i) {
      s1a = dot2(a[i].x, 0x3f803f80u, s1a); s2a = dot2(a[i].x, a[i].x, s2a);
      s1b = dot2(a[i].y, 0x3f803f80u, s1b); s2b = dot2(a[i].y, a[i].y, s2b);
      s1a = dot2(a[i].z, 0x3f803f80u, s1a); s2a = dot2(a[i].z, a[i].z, s2a);
      s1b = dot2(a[i].w, 0x3f803f80u, s1b); s2b = dot2(a[i].w, a[i].w, s2b);
    }
    float s1 = s1a + s1b, s2 = s2a + s2b;
    s1 += dppf<0x128>(s1); s2 += dppf<0x128>(s2);
    s1 = sum_q(s1); s2 = sum_q(s2);
    const LnC lc = ln_coef<false>(s1, s2);
    if (kq == 0) *(LAS f32x2 *)(c.L + L_ST + tt * 8) = f32x2{lc.A, lc.B};
  }
  __syncthreads();
}

template <int MODE>
DI void gemm_dir(const Ctx &c, int src, int tile, int lw, int jm, f32x4 &r0, f32x4 &r1, LnC &lc, v4u &xk) {
  const int lane = c.lane;
  v4u xf[14];
  src = opaque(src + tile * TILE_D);
#pragma unroll
  for (int ks = 0; ks < 14; ++ks) xf[ks] = LD16C(c.rs, lane * 16, src, ks * 1024);
  const LAS unsigned char *wb_ = c.L + lw + lane * 16;
  v4u wf[3][2];
#pragma unroll
  for (int ks = 0; ks < 2; ++ks) { wf[ks][0] = *(const LAS v4u *)(wb_ + ks * 1024); wf[ks][1] = *(const LAS v4u *)(wb_ + (14 + ks) * 1024); }
  SB();
  r0 = f32x4{0.f, 0.f, 0.f, 0.f}; r1 = r0;
  f32x4 ss = r0, gg = r0;
#pragma unroll
  for (int ks = 0; ks < 14; ++ks) {
    if (ks + 2 < 14) {
      wf[(ks + 2) % 3][0] = *(const LAS v4u *)(wb_ + (ks + 2) * 1024);
      wf[(ks + 2) % 3][1] = *(const LAS v4u *)(wb_ + (16 + ks) * 1024);
    }
    SB();
    r0 = mfma32(wf[ks % 3][0], xf[ks], r0);
    r1 = mfma32(wf[ks % 3][1], xf[ks], r1);
    if (MODE == 2) {
      ss = mfma32(v4u{0x3f803f80u, 0x3f803f80u, 0x3f803f80u, 0x3f803f80u}, xf[ks], ss);
      gg = mfma32(xf[ks], xf[ks], gg);
      if (ks == jm) xk = xf[ks];
    }
    SB();
  }
  SB();
  if (MODE == 2) lc = ln_coef<true>(ss[0], gram_diag(gg, lane));
}
DI v4u add_res(v4u x, f32x4 a, f32x4 b) {
  v4u o;
  o.x = pk2(bflo(x.x) + rbf(a[0]), bfhi(x.x) + rbf(a[1])); o.y = pk2(bflo(x.y) + rbf(a[2]), bfhi(x.y) + rbf(a[3]));
  o.z = pk2(bflo(x.z) + rbf(b[0]), bfhi(x.z) + rbf(b[1])); o.w = pk2(bflo(x.w) + rbf(b[2]), bfhi(x.w) + rbf(b[3]));
  return o;
}

DI void dual_softmax(f32x4 p0, f32x4 p1, bool v1, v4u &pos, v4u &neg) {
  float pv[8];
#pragma unroll
  for (int i = 0; i < 4; ++i) { pv[i] = rbf(p0[i]) * LOG2E; pv[4 + i] = rbf(p1[i]) * LOG2E; }
  float mx = fmaxf(fmaxf(pv[0], pv[1]), fmaxf(pv[2], pv[3]));
  float mn = fminf(fminf(pv[0], pv[1]), fminf(pv[2], pv[3]));
  const float mx1 = fmaxf(fmaxf(pv[4], pv[5]), fmaxf(pv[6], pv[7]));
  const float mn1 = fminf(fminf(pv[4], pv[5]), fminf(pv[6], pv[7]));
  if (v1) { mx = fmaxf(mx, mx1); mn = fminf(mn, mn1); }
  mx = max_q(mx); mn = min_q(mn);
  float ep[8], en[8];
#pragma unroll
  for (int i = 0; i < 8; ++i) { ep[i] = ex2(pv[i] - mx); en[i] = ex2(mn - pv[i]); }
  float sp = (ep[0] + ep[1]) + (ep[2] + ep[3]), sn = (en[0] + en[1]) + (en[2] + en[3]);
  const float sp1 = (ep[4] + ep[5]) + (ep[6] + ep[7]), sn1 = (en[4] + en[5]) + (en[6] + en[7]);
  if (v1) { sp += sp1; sn += sn1; }
  sp = sum_q(sp); sn = sum_q(sn);
  const float rp = rcp(sp), rn = rcp(sn);
  pos.x = pk2(ep[0] * rp, ep[1] * rp); pos.y = pk2(ep[2] * rp, ep[3] * rp);
  neg.x = pk2(en[0] * rn, en[1] * rn); neg.y = pk2(en[2] * rn, en[3] * rn);
  pos.z = v1 ? pk2(ep[4] * rp, ep[5] * rp) : 0u; pos.w = v1 ? pk2(ep[6] * rp, ep[7] * rp) : 0u;
  neg.z = v1 ? pk2(en[4] * rn, en[5] * rn) : 0u; neg.w = v1 ? pk2(en[6] * rn, en[7] * rn) : 0u;
}
DI v2u trr(const LAS unsigned char *a) {
  return __builtin_bit_cast(v2u, __builtin_amdgcn_ds_read_tr16_b64_v4i16((LAS bf16x4 *)a));
}

__global__ void __launch_bounds__(NTHR, 2) k_main(Params p) {
  extern __shared__ __attribute__((aligned(16))) unsigned char smem[];
  Ctx c;
  c.rs = __builtin_amdgcn_make_buffer_rsrc((void *)p.ws, 0, WS_END, 0x00020000);
  c.ctrl = (gu32 *)p.ws;
  c.L = (LAS unsigned char *)smem;
  c.tid = threadIdx.x; c.lane = c.tid & 63; c.w = __builtin_amdgcn_readfirstlane(c.tid >> 6);
  const int b = blockIdx.x, xcd = b & 7, r = b >> 3;
  const int g = 4 * xcd + (r >> 3), j = r & 7;
  const int tau = 2 * xcd + (r >> 4), head = r & 15;
  const int w = c.w;
  const int jm = r & 15;
  const int jmc = jm < 14 ? jm : 0;
  const f32x4 z4 = {0.f, 0.f, 0.f, 0.f};

  dma_run<56>(p, c, WQKV_T + head * (84 * 1024) + 28 * 1024, L_WQ + 28 * 1024);
  v4u xf[14];
  {
    const int xb = opaque(XIN + (tau * 8 + w) * TILE_D);
#pragma unroll
    for (int ks = 0; ks < 14; ++ks) xf[ks] = LD16C(c.rs, c.lane * 16, xb, ks * 1024);
  }
  dma_run<28>(p, c, WQKV_T + head * (84 * 1024), L_WQ);
  SB();
  v4u cpf;
  {
    unsigned long long pc;
    asm volatile("s_getpc_b64 %0" : "=s"(pc));
    const int idx = (r * 8 + w) % CODE_PF;
    cpf = *(const GAS v4u *)((pc & ~255ull) + idx * 1024 + c.lane * 16);
  }

  for (int pair = 0; pair < 2; ++pair) {
    const unsigned e1 = pair + 1;
    const int xin = XIN + pair * ACT_D;
    v4u wr[28];
    const int w3b = W3_T + (16 * (j < 7 ? j : 0) + 2 * w) * 14 * 1024;
    f32x4 pq0, pq1;
    {
      FRESH();
      const v4u wph0 = ld16(c.rs, lane * 16, WPHI_T + (head * 2 + 0) * 1024);
      const v4u wph1 = ld16(c.rs, lane * 16, WPHI_T + (head * 2 + 1) * 1024);
      v4u cs[6];
#pragma unroll
      for (int nt = 0; nt < 6; ++nt) cs[nt] = ld16(c.rs, q4 * 16 + nt * 64, CS_QKV + head * 384);
      if (pair > 0) {
        DRAIN();
        wait2(c, C_XN + 2 * tau, C_XN + 2 * tau + 1, 7u * pair);
        const int xb = opaque(xin + (tau * 8 + w) * TILE_D);
#pragma unroll
        for (int ks = 0; ks < 14; ++ks) xf[ks] = LD16C(c.rs, lane * 16, xb, ks * 1024);
      }

      if (pair == 0) {
        DRAIN();
        __syncthreads();
        asm volatile("" :: "v"(cpf));
      }
      const LAS unsigned char *wb_ = c.L + L_WQ + lane * 16;
      f32x4 ss = z4, gg = z4;
      f32x4 acc[4];
#pragma unroll
      for (int nt = 0; nt < 4; ++nt) acc[nt] = z4;
      {
        v4u wf[2][4];
#pragma unroll
        for (int nt = 0; nt < 4; ++nt) wf[0][nt] = *(const LAS v4u *)(wb_ + ((2 + nt) * 14) * 1024);
        SB();
#pragma unroll
        for (int ks = 0; ks < 14; ++ks) {
          if (ks + 1 < 14) {
#pragma unroll
            for (int nt = 0; nt < 4; ++nt) wf[(ks + 1) & 1][nt] = *(const LAS v4u *)(wb_ + ((2 + nt) * 14 + ks + 1) * 1024);
          }
          SB();
#pragma unroll
          for (int nt = 0; nt < 4; ++nt) acc[nt] = mfma32(wf[ks & 1][nt], xf[ks], acc[nt]);
          ss = mfma32(v4u{0x3f803f80u, 0x3f803f80u, 0x3f803f80u, 0x3f803f80u}, xf[ks], ss);
          gg = mfma32(xf[ks], xf[ks], gg);
          SB();
        }
      }

      const LnC lc = ln_coef<false>(ss[0], gram_diag(gg, lane));
      const bool v1 = q4 < 3;
      {
        v2u kv_[4];
#pragma unroll
        for (int nt = 0; nt < 4; ++nt) {
          const f32x4 v = acc[nt] * lc.A - __builtin_bit_cast(f32x4, cs[2 + nt]) * lc.B;
          kv_[nt] = v2u{pk2(v[0], v[1]), pk2(v[2], v[3])};
        }
        const v4u kp = {kv_[0].x, kv_[0].y, kv_[1].x, kv_[1].y};
        v4u pp, pn;
        dual_softmax(mfma32(wph0, kp, z4), mfma32(wph1, kp, z4), v1, pp, pn);
        LAS unsigned char *pk = c.L + L_PK + (16 * w + t16) * PK_STR + q4 * 8;
        *(LAS v2u *)(pk) = v2u{pp.x, pp.y};
        *(LAS v2u *)(pk + 32) = v2u{pp.z, pp.w};
        *(LAS v2u *)(pk + 64) = v2u{pn.x, pn.y};
        *(LAS v2u *)(pk + 96) = v2u{pn.z, pn.w};
        LAS unsigned char *vv = c.L + L_V + (16 * w + t16) * V_STR + q4 * 8;
        *(LAS v2u *)(vv) = kv_[2];
        *(LAS v2u *)(vv + 32) = v1 ? kv_[3] : v2u{0x00003f80u, 0u};
      }
      __syncthreads();

      {
        const int ft = w >> 1, dt = w & 1;
        const int rq = (lane & 15) >> 2, rp = lane & 3;
        const LAS unsigned char *pa = c.L + L_PK + (8 * q4 + rq) * PK_STR + (16 * ft + 4 * rp) * 2;
        const LAS unsigned char *pb = c.L + L_V + (8 * q4 + rq) * V_STR + (16 * dt + 4 * rp) * 2;
        f32x4 kv = z4;
#pragma unroll
        for (int ks = 0; ks < 4; ++ks) {
          const v2u a0 = trr(pa + (32 * ks) * PK_STR), a1 = trr(pa + (32 * ks + 4) * PK_STR);
          const v2u b0 = trr(pb + (32 * ks) * V_STR), b1 = trr(pb + (32 * ks + 4) * V_STR);
          kv = mfma32(v4u{a0.x, a0.y, a1.x, a1.y}, v4u{b0.x, b0.y, b1.x, b1.y}, kv);
        }
        st8(c.rs, lane * 16 + (tau & 1) * 8, KVP_B + ((pair * 16 + head) * 8 + (tau >> 1)) * 8192 + w * 1024, v2u{pk2(kv[0], kv[1]), pk2(kv[2], kv[3])});
      }

      publish(c, C_KV + head);

      dma_run<28>(p, c, WO_T + jmc * (28 * 1024), L_WO);
      {
        f32x4 q0 = z4, q1 = z4;
        v4u wf[3][2];
#pragma unroll
        for (int ks = 0; ks < 2; ++ks) { wf[ks][0] = *(const LAS v4u *)(wb_ + ks * 1024); wf[ks][1] = *(const LAS v4u *)(wb_ + (14 + ks) * 1024); }
        SB();
#pragma unroll
        for (int ks = 0; ks < 14; ++ks) {
          if (ks + 2 < 14) {
            wf[(ks + 2) % 3][0] = *(const LAS v4u *)(wb_ + (ks + 2) * 1024);
            wf[(ks + 2) % 3][1] = *(const LAS v4u *)(wb_ + (16 + ks) * 1024);
          }
          SB();
          q0 = mfma32(wf[ks % 3][0], xf[ks], q0);
          q1 = mfma32(wf[ks % 3][1], xf[ks], q1);
          SB();
        }
        q0 = q0 * lc.A - __builtin_bit_cast(f32x4, cs[0]) * lc.B;
        q1 = q1 * lc.A - __builtin_bit_cast(f32x4, cs[1]) * lc.B;
        const v4u qp = {pk2(q0[0], q0[1]), pk2(q0[2], q0[3]), pk2(q1[0], q1[1]), pk2(q1[2], q1[3])};
        pq0 = mfma32(wph0, qp, z4); pq1 = mfma32(wph1, qp, z4);
      }
#pragma unroll
      for (int ks = 0; ks < 14; ++ks) xf[ks] = v4u{0u, 0u, 0u, 0u};
    }
    {
      FRESH();
      wait1(c, C_KV + head, 16u * e1);

      v4u pr[8];
      const int kb = opaque(KVP_B + (pair * 16 + head) * 8 * 8192 + w * 1024);
#pragma unroll
      for (int t = 0; t < 8; ++t) pr[t] = ld16(c.rs, lane * 16, kb + t * 8192);
      SB();
      v4u phq_p, phq_n;
      dual_softmax(pq0, pq1, q4 < 3, phq_p, phq_n);
      SB();
      f32x4 s = z4;
#pragma unroll
      for (int t = 0; t < 8; ++t) {
        s += f32x4{bflo(pr[t].x), bfhi(pr[t].x), bflo(pr[t].y), bfhi(pr[t].y)};
        s += f32x4{bflo(pr[t].z), bfhi(pr[t].z), bflo(pr[t].w), bfhi(pr[t].w)};
      }
      {
        const int ft = w >> 1, dt = w & 1, d = 16 * dt + t16, part = ft >> 1, half = ft & 1;
        *(LAS v2u *)(c.L + L_KVS + ((d * 2 + part) * 4 + q4) * 16 + half * 8) = v2u{pk2(s[0], s[1]), pk2(s[2], s[3])};
      }
      __syncthreads();
      f32x4 nm[2];
#pragma unroll
      for (int dt = 0; dt < 2; ++dt) {
        const v4u ap = *(const LAS v4u *)(c.L + L_KVS + (((16 * dt + t16) * 2 + 0) * 4 + q4) * 16);
        const v4u an = *(const LAS v4u *)(c.L + L_KVS + (((16 * dt + t16) * 2 + 1) * 4 + q4) * 16);
        nm[dt] = mfma32(ap, phq_p, z4);
        nm[dt] = mfma32(an, phq_n, nm[dt]);
      }
      const float den0 = __shfl(nm[1][0], 48 + t16);
      const float rden = rcp(rbf(rbf(den0) + 1e-6f));
#pragma unroll
      for (int dt = 0; dt < 2; ++dt) {
        if (dt == 0 || q4 < 3) {
          const int col = DH * head + 16 * dt + 4 * q4;
          v2u o;
          o.x = pk2(rbf(nm[dt][0]) * rden, rbf(nm[dt][1]) * rden);
          o.y = pk2(rbf(nm[dt][2]) * rden, rbf(nm[dt][3]) * rden);
          st8(c.rs, (col >> 3) * 256 + t16 * 16 + ((col >> 2) & 1) * 8, O_B + pair * ACT_D + (tau * 8 + w) * TILE_D, o);
        }
      }

      publish(c, C_O + tau);

      dma_run<28>(p, c, W1_T + jmc * (28 * 1024), L_W1);
      dma_run<28>(p, c, W2_T + jmc * (28 * 1024), L_W2);
    }
    if (jm < 14) {
      const int tile = tau * 8 + w;
      v4u yk;
      {
        FRESH();
        const int eo = tile * TILE_D + jm * 1024;
        const v4u xr = ld16(c.rs, lane * 16, xin + eo);
        wait1(c, C_O + tau, 16u * e1);
        f32x4 r0, r1; LnC lc; v4u xk;
        gemm_dir<0>(c, O_B + pair * ACT_D, tile, L_WO, jm, r0, r1, lc, xk);

        st16(c.rs, lane * 16, XP_B + pair * ACT_D + eo, add_res(xr, r0, r1));
        publish(c, C_XP + tau);
        load_run<0, 10, 28>(c, w3b, wr);
      }
      {
        FRESH();
        const int eo = tile * TILE_D + jm * 1024;
        const f32x4 cs0 = __builtin_bit_cast(f32x4, ld16(c.rs, q4 * 32, CS_W1 + jm * 128));
        const f32x4 cs1 = __builtin_bit_cast(f32x4, ld16(c.rs, q4 * 32 + 16, CS_W1 + jm * 128));
        wait1(c, C_XP + tau, 14u * e1);
        f32x4 r0, r1; LnC lc; v4u xk;
        gemm_dir<2>(c, XP_B + pair * ACT_D, tile, L_W1, jm, r0, r1, lc, xk);

        r0 = r0 * lc.A - cs0 * lc.B;
        r1 = r1 * lc.A - cs1 * lc.B;
        {
          const int k8 = 4 * jm + q4, cg = k8 / 14;
          const int d = (cg == 0) ? 1 : (cg == 1) ? -1 : (cg == 2) ? 2 : -2;
          const int tok = 16 * tile + t16, sl = tok + d;
          const int ho = (sl < 0 || sl >= S_) ? 0x7ff00000 : (sl >> 4) * TILE_D + k8 * 256 + (sl & 15) * 16;
          st16(c.rs, ho, H_B + pair * ACT_D, v4u{gelu2(r0[0], r0[1]), gelu2(r0[2], r0[3]), gelu2(r1[0], r1[1]), gelu2(r1[2], r1[3])});
          const int src = tok - d;
          if (src < 0 || src >= S_) st16(c.rs, (tok >> 4) * TILE_D + k8 * 256 + (tok & 15) * 16, H_B + pair * ACT_D, v4u{0u, 0u, 0u, 0u});
        }
#pragma unroll
        for (int e = 0; e < 4; ++e) {
          const unsigned d = pk2(bflo(xk[e]) - lc.mb, bfhi(xk[e]) - lc.mb);
          yk[e] = pk2(bflo(d) * lc.r, bfhi(d) * lc.r);
        }
        publish(c, C_H + tau);
        load_run<10, 19, 28>(c, w3b, wr);
      }
      {
        FRESH();
        const int eo = tile * TILE_D + jm * 1024;
        wait3(c, C_H + tau, C_H + (tau > 0 ? tau - 1 : tau), C_H + (tau < 15 ? tau + 1 : tau), 14u * e1);
        f32x4 r0, r1; LnC lc; v4u xk;
        gemm_dir<0>(c, H_B + pair * ACT_D, tile, L_W2, jm, r0, r1, lc, xk);

        st16(c.rs, lane * 16, X1_B + pair * ACT_D + eo, add_res(yk, r0, r1));
        publish(c, C_X1 + tau);
        load_run<19, 28, 28>(c, w3b, wr);
      }
    } else {
      FRESH();
      load_run<0, 28, 28>(c, w3b, wr);
    }
    if (j < 7) {
      {
        FRESH();
        const f32x4 cs0 = __builtin_bit_cast(f32x4, ld16(c.rs, q4 * 32, CS_W3 + (256 * j + 32 * w) * 4));
        const f32x4 cs1 = __builtin_bit_cast(f32x4, ld16(c.rs, q4 * 32 + 16, CS_W3 + (256 * j + 32 * w) * 4));
        wait1(c, C_X1 + (g >> 1), 14u * e1);
        {
          v4u a[7];
          stage_issue(c, X1_B + pair * ACT_D, g, a);
          stage_finish(c, a);
        }
        f32x4 acc[4][2];
#pragma unroll
        for (int m = 0; m < 4; ++m) { acc[m][0] = z4; acc[m][1] = z4; }
        const int w4b = opaque(W4_T + (4 * j * 56 + 7 * w) * 1024);
        {
          v4u bf[2][4];
          const LAS unsigned char *ab = c.L + L_A + lane * 16;
#pragma unroll
          for (int m = 0; m < 4; ++m) bf[0][m] = *(const LAS v4u *)(ab + m * TILE_D);
#pragma unroll
          for (int ks = 0; ks < 14; ++ks) {
            if (ks + 1 < 14) {
#pragma unroll
              for (int m = 0; m < 4; ++m) bf[(ks + 1) & 1][m] = *(const LAS v4u *)(ab + m * TILE_D + (ks + 1) * 1024);
            }
            SB();
#pragma unroll
            for (int m = 0; m < 4; ++m) {
              acc[m][0] = mfma32(wr[ks], bf[ks & 1][m], acc[m][0]);
              acc[m][1] = mfma32(wr[14 + ks], bf[ks & 1][m], acc[m][1]);
            }
            SB();
            wr[ks] = LD16C(c.rs, lane * 16, w4b, ((ks / 7) * 56 + ks % 7) * 1024);
            wr[14 + ks] = LD16C(c.rs, lane * 16, w4b, ((2 + ks / 7) * 56 + ks % 7) * 1024);
          }
          SB();
        }
#pragma unroll
        for (int m = 0; m < 4; ++m) {
          const f32x2 st = *(const LAS f32x2 *)(c.L + L_ST + (16 * m + t16) * 8);
          const f32x4 u0 = acc[m][0] * st.x - cs0 * st.y, u1 = acc[m][1] * st.x - cs1 * st.y;
          const v4u o = {gelu2(u0[0], u0[1]), gelu2(u0[2], u0[3]), gelu2(u1[0], u1[1]), gelu2(u1[2], u1[3])};
          st16(c.rs, lane * 16, G_B + pair * ACT_F + (4 * g + m) * TILE_F + (32 * j + 4 * w) * 256, o);
        }
        publish(c, C_G + g);
      }
      {
        FRESH();
        const int roff = q4 * TILE_D + t16 * 16;
        const int rso = 4 * g * TILE_D + (8 * j + w) * 256;
        const v4u rxp = ld16(c.rs, roff, XP_B + pair * ACT_D + rso);
        const v4u rx1 = ld16(c.rs, roff, X1_B + pair * ACT_D + rso);
        wait1(c, C_G + g, 7u * e1);
        const int abase = opaque(G_B + pair * ACT_F + 4 * g * TILE_F + 7 * w * 1024);
        constexpr int RING = 14;
        v4u a[RING];
#pragma unroll
        for (int i = 0; i < RING; ++i) a[i] = LD16C(c.rs, lane * 16, abase, (i / 7) * TILE_F + (i % 7) * 1024);
        SB();
#pragma unroll
        for (int m = 0; m < 4; ++m) {
          f32x4 acc[4];
#pragma unroll
          for (int t = 0; t < 4; ++t) acc[t] = z4;
#pragma unroll
          for (int kk = 0; kk < 7; ++kk) {
            const int i = m * 7 + kk;
            const v4u cur = a[i % RING];
            if (i + RING < 28) a[i % RING] = LD16C(c.rs, lane * 16, abase, ((i + RING) / 7) * TILE_F + ((i + RING) % 7) * 1024);
#pragma unroll
            for (int t = 0; t < 4; ++t) acc[t] = mfma32(wr[t * 7 + kk], cur, acc[t]);
            SB();
          }
#pragma unroll
          for (int t = 0; t < 4; ++t) *(LAS f32x4 *)(c.L + L_RED + ((w * 16 + m * 4 + t) * 64 + lane) * 16) = acc[t];
        }
        if (pair == 0) {
          const int qb = opaque(WQKV_T + head * (84 * 1024) + w * 1024);
#pragma unroll
          for (int i = 0; i < 11; ++i) wr[i] = ld16(c.rs, lane * 16, qb + (w + 8 * i < 84 ? i * 8192 : 0));
          SB();
        }

        __syncthreads();
        f32x4 s0 = z4, s1 = z4;
        {
          const int t = w >> 1, qq = 2 * (w & 1);
#pragma unroll
          for (int ww = 0; ww < 8; ++ww) {
            s0 += *(const LAS f32x4 *)(c.L + L_RED + ((ww * 16 + q4 * 4 + t) * 64 + t16 + 16 * qq) * 16);
            s1 += *(const LAS f32x4 *)(c.L + L_RED + ((ww * 16 + q4 * 4 + t) * 64 + t16 + 16 * (qq + 1)) * 16);
          }
        }
        v4u o;
        o.x = pk2(bflo(rxp.x) + rbf(bflo(rx1.x) + rbf(s0[0])), bfhi(rxp.x) + rbf(bfhi(rx1.x) + rbf(s0[1])));
        o.y = pk2(bflo(rxp.y) + rbf(bflo(rx1.y) + rbf(s0[2])), bfhi(rxp.y) + rbf(bfhi(rx1.y) + rbf(s0[3])));
        o.z = pk2(bflo(rxp.z) + rbf(bflo(rx1.z) + rbf(s1[0])), bfhi(rxp.z) + rbf(bfhi(rx1.z) + rbf(s1[1])));
        o.w = pk2(bflo(rxp.w) + rbf(bflo(rx1.w) + rbf(s1[2])), bfhi(rxp.w) + rbf(bfhi(rx1.w) + rbf(s1[3])));

        if (pair == 0) {
          st16(c.rs, roff, XIN + ACT_D + rso, o);
          publish(c, C_XN + g);
#pragma unroll
          for (int i = 0; i < 11; ++i)
            if (w + 8 * i < 84) *(LAS v4u *)(c.L + L_WQ + (w + 8 * i) * 1024 + lane * 16) = wr[i];
        } else {
          *(v4u *)(p.out + (size_t)(64 * g + 16 * q4 + t16) * D_ + 64 * j + 8 * w) = o;
        }

      }
    }
    else if (pair == 0) {
      DRAIN();
      __syncthreads();
      c.lane = opaque_v(c.lane);
      dma_run<84>(p, c, WQKV_T + head * (84 * 1024), L_WQ);
    }
  }
}

}

extern "C" void kernel_launch(void *const *d_in, const int *in_sizes, int n_in, void *d_out, int out_size, void *d_ws,
                              size_t ws_size, hipStream_t stream) {
  Params p;
  p.x = (const unsigned short *)d_in[0];
  p.wqkv = (const unsigned short *)d_in[1];
  p.wphi = (const unsigned short *)d_in[2];
  p.wo = (const unsigned short *)d_in[3];
  p.w1 = (const unsigned short *)d_in[5];
  p.w2 = (const unsigned short *)d_in[6];
  p.w3 = (const unsigned short *)d_in[7];
  p.w4 = (const unsigned short *)d_in[8];
  p.out = (unsigned short *)d_out;
  p.ws = (unsigned char *)d_ws;
  (void)hipFuncSetAttribute((const void *)k_main, hipFuncAttributeMaxDynamicSharedMemorySize, LDS_BYTES);
  hipLaunchKernelGGL(k_prep, dim3(PREP_BLOCKS), dim3(256), 0, stream, p);
  hipLaunchKernelGGL(k_main, dim3(256), dim3(NTHR), LDS_BYTES, stream, p);
}
```
